```python
import torch

def _softmax(x, axis=-1):
    return torch.softmax(x, dim=axis)
def _gelu(x):
    return torch.nn.functional.gelu(x, approximate="tanh")
def _silu(x):
    return torch.nn.functional.silu(x)
def _sigmoid(x):
    return torch.sigmoid(x)
def _layernorm(x, axis=-1, eps=1e-5):
    m = x.mean(dim=axis, keepdim=True)
    v = x.var(dim=axis, keepdim=True, unbiased=False)
    return (x - m) / torch.sqrt(v + eps)
def _scatter_add(tgt, idx, src, axis=0):
    return tgt.clone().index_add_(axis, idx, src)
def _segment_sum(src, seg, n):
    out = torch.zeros((n,) + src.shape[1:], dtype=src.dtype)
    return out.index_add_(0, seg, src)
def _pad(x, pw):
    flat = tuple(v for pair in reversed(pw) for v in pair)
    return torch.nn.functional.pad(x, flat)

kernel_name = 'sequential_preLN__linear_kernel_hedgehog__swiglu_ffn__prompt'

def setup_inputs():

    _w = (lambda a: (lambda t: t.to(torch.bfloat16) if t.is_floating_point() else t)(torch.as_tensor(a)))
    def _A_setup():
        B, S, D, H = 1, 2048, 576, 32
        Dh = D // H
        return {
            "x": _w((lambda *s: torch.randn(*s, dtype=torch.bfloat16) * 0.05)(B, S, D)),
            "wqkv": _w((lambda *s: torch.randn(*s, dtype=torch.bfloat16) * 0.2)(D, 3 * D)),
            "w_phi": _w((lambda *s: torch.randn(*s, dtype=torch.bfloat16) * 0.2)(H, Dh, Dh) * (Dh ** -0.5)),
            "wo": _w((lambda *s: torch.randn(*s, dtype=torch.bfloat16) * 0.05)(D, D)),
        }
    _A_in = _A_setup()

    def _B_setup():
        B, S, D, F = 1, 2048, 576, 4608
        return {
            "x": _w((lambda *s: torch.randn(*s, dtype=torch.bfloat16) * 0.05)(B, S, D)),
            "w_gate": _w((lambda *s: torch.randn(*s, dtype=torch.bfloat16) * 0.05)(D, F)),
            "w_up": _w((lambda *s: torch.randn(*s, dtype=torch.bfloat16) * 0.05)(D, F)),
            "w_down": _w((lambda *s: torch.randn(*s, dtype=torch.bfloat16) * 0.05)(F, D)),
        }
    _B_in = _B_setup()

    return {
        "A_x": _A_in["x"],
        "A_wqkv": _A_in["wqkv"],
        "A_w_phi": _A_in["w_phi"],
        "A_wo": _A_in["wo"],
        "B_x": _B_in["x"],
        "B_w_gate": _B_in["w_gate"],
        "B_w_up": _B_in["w_up"],
        "B_w_down": _B_in["w_down"],
    }

def reference(A_x, A_wqkv, A_w_phi, A_wo, B_x, B_w_gate, B_w_up, B_w_down):
    def _A_ref(x, wqkv, w_phi, wo):
        B, S, D = x.shape
        H = 32; Dh = D // H
        qkv = (lambda x, *d: x.permute(*d))((x @ wqkv).reshape(B, S, 3, H, Dh), 2, 0, 3, 1, 4)
        q, k, v = qkv[0], qkv[1], qkv[2]
        pq = torch.einsum("bhsd,hdf->bhsf", q, w_phi)
        pk = torch.einsum("bhsd,hdf->bhsf", k, w_phi)
        phi_q = torch.concatenate([_softmax(pq, axis=-1), _softmax(-pq, axis=-1)], axis=-1)
        phi_k = torch.concatenate([_softmax(pk, axis=-1), _softmax(-pk, axis=-1)], axis=-1)
        kv = torch.einsum("bhsf,bhsd->bhfd", phi_k, v)
        z  = torch.einsum("bhsf->bhf", phi_k)
        num = torch.einsum("bhqf,bhfd->bhqd", phi_q, kv)
        den = torch.einsum("bhqf,bhf->bhq", phi_q, z)[..., None] + 1e-6
        o = num / den
        return (lambda x, *d: x.permute(*d))(o, 0, 2, 1, 3).reshape(B, S, D) @ wo

    def _B_ref(x, w_gate, w_up, w_down):
        g = _silu(x @ w_gate)
        u = x @ w_up
        return (g * u) @ w_down

    x = A_x
    h = x + _A_ref(_layernorm(x, axis=-1), A_wqkv, A_w_phi, A_wo)
    h2 = h + _B_ref(_layernorm(h, axis=-1), B_w_gate, B_w_up, B_w_down)
    return h2
```

```cpp
#include <hip/hip_runtime.h>
#include <hip/hip_bf16.h>
typedef unsigned short u16;
__device__ __forceinline__ float bf2f(u16 v){ return __uint_as_float(((unsigned)v)<<16); }
__device__ __forceinline__ u16 f2bf(float f){ unsigned u=__float_as_uint(f); u += 0x7fffu + ((u>>16)&1u); return (u16)(u>>16); }
__device__ __forceinline__ float rbf(float f){ return bf2f(f2bf(f)); }
constexpr int S=2048, D=576, H=32, DH=18, F=4608;

__global__ void ln_k(const u16* x, u16* y){
  int row=blockIdx.x; int lane=threadIdx.x;
  float v[9]; float s=0;
  for(int i=0;i<9;i++){ v[i]=bf2f(x[row*D+lane+64*i]); s+=v[i]; }
  for(int o=32;o;o>>=1) s+=__shfl_xor(s,o);
  float m=s/D; float q=0;
  for(int i=0;i<9;i++){ float d=v[i]-m; q+=d*d; }
  for(int o=32;o;o>>=1) q+=__shfl_xor(q,o);
  float r=rsqrtf(q/D+1e-5f);
  for(int i=0;i<9;i++) y[row*D+lane+64*i]=f2bf((v[i]-m)*r);
}
__global__ void gemm_k(const u16* A,const u16* W,float* C,int M,int N,int K){
  __shared__ float sA[64][17], sW[16][65];
  int tx=threadIdx.x&15, ty=threadIdx.x>>4;
  int bm=blockIdx.y*64, bn=blockIdx.x*64;
  float acc[4][4]={};
  for(int k0=0;k0<K;k0+=16){
    for(int e=threadIdx.x;e<64*16;e+=256){ int r=e/16,c=e%16; sA[r][c]=bf2f(A[(long)(bm+r)*K+k0+c]); }
    for(int e=threadIdx.x;e<64*16;e+=256){ int r=e/64,c=e%64; sW[r][c]=(bn+c<N)?bf2f(W[(long)(k0+r)*N+bn+c]):0.f; }
    __syncthreads();
    for(int k=0;k<16;k++){
      float a[4],b[4];
      for(int i=0;i<4;i++){a[i]=sA[ty*4+i][k]; b[i]=sW[k][tx*4+i];}
      for(int i=0;i<4;i++)for(int j=0;j<4;j++)acc[i][j]+=a[i]*b[j];
    }
    __syncthreads();
  }
  for(int i=0;i<4;i++)for(int j=0;j<4;j++){ int c=bn+tx*4+j; if(c<N) C[(long)(bm+ty*4+i)*N+c]=acc[i][j]; }
}
__global__ void phi_k(const float* qkv,const u16* wphi,float* phq,float* phk){
  int idx=blockIdx.x*blockDim.x+threadIdx.x;
  int which=idx&1; int h=(idx>>1)%H; int s=(idx>>1)/H;
  const float* src=qkv+(long)s*3*D+which*D+h*DH;
  float p[DH];
  for(int f=0;f<DH;f++){ float a=0; for(int d=0;d<DH;d++) a+=rbf(src[d])*bf2f(wphi[(h*DH+d)*DH+f]); p[f]=a; }
  float mx=p[0],mn=p[0]; for(int f=1;f<DH;f++){mx=fmaxf(mx,p[f]);mn=fminf(mn,p[f]);}
  float s1=0,s2=0; float e1[DH],e2[DH];
  for(int f=0;f<DH;f++){e1[f]=__expf(p[f]-mx);s1+=e1[f];e2[f]=__expf(mn-p[f]);s2+=e2[f];}
  float* dst=(which?phk:phq)+((long)s*H+h)*2*DH;
  for(int f=0;f<DH;f++){dst[f]=e1[f]/s1;dst[DH+f]=e2[f]/s2;}
}
__global__ void kv_k(const float* qkv,const float* phk,float* kv){
  int h=blockIdx.x; int t=threadIdx.x; if(t>=36*19) return;
  int f=t/19,d=t%19; float a=0;
  for(int s=0;s<S;s++){ float p=rbf(phk[((long)s*H+h)*36+f]); float v=(d<18)?rbf(qkv[(long)s*3*D+2*D+h*DH+d]):1.f; a+=p*v; }
  kv[(h*36+f)*19+d]=a;
}
__global__ void o_k(const float* phq,const float* kv,u16* o){
  int idx=blockIdx.x*blockDim.x+threadIdx.x;
  int h=idx%H,s=idx/H;
  const float* p=phq+((long)s*H+h)*36;
  float den=1e-6f; for(int f=0;f<36;f++) den+=p[f]*kv[(h*36+f)*19+18];
  for(int d=0;d<18;d++){ float a=0; for(int f=0;f<36;f++) a+=p[f]*kv[(h*36+f)*19+d]; o[(long)s*D+h*DH+d]=f2bf(a/den); }
}
__global__ void add_k(const u16* x,const float* y,u16* o,int n){ int i=blockIdx.x*blockDim.x+threadIdx.x; if(i<n) o[i]=f2bf(bf2f(x[i])+y[i]); }
__global__ void glu_k(const float* g,const float* u,u16* o,int n){ int i=blockIdx.x*blockDim.x+threadIdx.x; if(i<n){ float a=g[i]; o[i]=f2bf(a/(1.f+__expf(-a))*u[i]); } }

extern "C" void kernel_launch(void* const* d_in, const int* in_sizes, int n_in, void* d_out, int out_size, void* d_ws, size_t ws_size, hipStream_t stream){
  const u16* x=(const u16*)d_in[0]; const u16* wqkv=(const u16*)d_in[1]; const u16* wphi=(const u16*)d_in[2]; const u16* wo=(const u16*)d_in[3];
  const u16* wg=(const u16*)d_in[5]; const u16* wu=(const u16*)d_in[6]; const u16* wd=(const u16*)d_in[7];
  char* ws=(char*)d_ws; size_t off=0; auto al=[&](size_t n){ char* p=ws+off; off+=(n+255)&~255ull; return p; };
  u16* xn=(u16*)al(S*D*2); float* qkv=(float*)al((size_t)S*3*D*4); float* phq=(float*)al((size_t)S*H*36*4); float* phk=(float*)al((size_t)S*H*36*4);
  float* kv=(float*)al(H*36*19*4); u16* o=(u16*)al(S*D*2); float* tmp=(float*)al((size_t)S*D*4); u16* h=(u16*)al(S*D*2);
  float* g=(float*)al((size_t)S*F*4); float* u=(float*)al((size_t)S*F*4); u16* hh=(u16*)al((size_t)S*F*2);
  ln_k<<<S,64,0,stream>>>(x,xn);
  gemm_k<<<dim3(3*D/64,S/64),256,0,stream>>>(xn,wqkv,qkv,S,3*D,D);
  phi_k<<<S*H*2/256,256,0,stream>>>(qkv,wphi,phq,phk);
  kv_k<<<H,704,0,stream>>>(qkv,phk,kv);
  o_k<<<S*H/256,256,0,stream>>>(phq,kv,o);
  gemm_k<<<dim3(D/64,S/64),256,0,stream>>>(o,wo,tmp,S,D,D);
  add_k<<<S*D/256,256,0,stream>>>(x,tmp,h,S*D);
  ln_k<<<S,64,0,stream>>>(h,xn);
  gemm_k<<<dim3(F/64,S/64),256,0,stream>>>(xn,wg,g,S,F,D);
  gemm_k<<<dim3(F/64,S/64),256,0,stream>>>(xn,wu,u,S,F,D);
  glu_k<<<S*F/256,256,0,stream>>>(g,u,hh,S*F);
  gemm_k<<<dim3(D/64,S/64),256,0,stream>>>(hh,wd,tmp,S,D,F);
  add_k<<<S*D/256,256,0,stream>>>(h,tmp,(u16*)d_out,S*D);
}
```

```cpp
#include <hip/hip_runtime.h>
#include <hip/hip_bf16.h>
#include <type_traits>
typedef unsigned short u16;
typedef __attribute__((ext_vector_type(4))) short s16x4;
typedef __attribute__((ext_vector_type(8))) short s16x8;
typedef __attribute__((ext_vector_type(8))) __bf16 bf16x8;
typedef __attribute__((ext_vector_type(2))) __bf16 bf16x2;
typedef __attribute__((ext_vector_type(2))) float f32x2;
typedef __attribute__((ext_vector_type(2))) unsigned u32x2;
typedef __attribute__((ext_vector_type(4))) unsigned u32x4;
typedef __attribute__((ext_vector_type(16))) float f32x16;
#define LAS __attribute__((address_space(3)))
#define GAS __attribute__((address_space(1)))
#define DI __device__ __forceinline__
#define RLX_AGENT __ATOMIC_RELAXED, __HIP_MEMORY_SCOPE_AGENT

#define STAMP(i)
#define STAMP_DECL
#define STAMP_OUT(k)
constexpr int S=2048, D=576, H=32, DH=18, F=4608;

template<int I,int N,class Fn> DI void static_for(Fn&& f){ if constexpr(I<N){ f(std::integral_constant<int,I>{}); static_for<I+1,N>(f);} }
DI float bf2f(u16 v){ return __uint_as_float(((unsigned)v)<<16); }
DI u16 f2bf(float f){ unsigned u=__float_as_uint(f); u += 0x7fffu + ((u>>16)&1u); return (u16)(u>>16); }
DI float rbf(float f){ return bf2f(f2bf(f)); }
DI unsigned pk2(float a,float b){ f32x2 f={a,b}; return __builtin_bit_cast(unsigned,__builtin_convertvector(f,bf16x2)); }
DI float lo16(unsigned u){ return __uint_as_float(u<<16); }
DI float hi16(unsigned u){ return __uint_as_float(u&0xffff0000u); }
#define MFMA32(a,b,c) __builtin_amdgcn_mfma_f32_32x32x16_bf16((a),(b),(c),0,0,0)
template<int N> DI void wait_vm(){ asm volatile("s_waitcnt vmcnt(%0)"::"n"(N):"memory"); }
DI void wait_lgkm0(){ asm volatile("s_waitcnt lgkmcnt(0)":::"memory"); }
DI void bar(){ __builtin_amdgcn_s_barrier(); }
template<int TS> DI bf16x8 ldtr(LAS char* p){
  s16x4 a=__builtin_amdgcn_ds_read_tr16_b64_v4i16((LAS s16x4*)p);
  s16x4 b=__builtin_amdgcn_ds_read_tr16_b64_v4i16((LAS s16x4*)(p+TS));
  return __builtin_bit_cast(bf16x8,__builtin_shufflevector(a,b,0,1,2,3,4,5,6,7));
}
DI void gld4x4(u32x4& a,u32x4& b,u32x4& c,u32x4& d,unsigned voff,const char* sb){
  asm volatile("global_load_dwordx4 %0, %4, %5\n\tglobal_load_dwordx4 %1, %4, %5 offset:1024\n\tglobal_load_dwordx4 %2, %4, %5 offset:2048\n\tglobal_load_dwordx4 %3, %4, %5 offset:3072"
      :"=&v"(a),"=&v"(b),"=&v"(c),"=&v"(d):"v"(voff),"s"(sb):"memory"); }
template<int N> DI void wait_vm4n(u32x4& a,u32x4& b,u32x4& c,u32x4& d){ asm volatile("s_waitcnt vmcnt(%4)":"+v"(a),"+v"(b),"+v"(c),"+v"(d):"n"(N):"memory"); }
template<int N> DI void wait_vm4(u32x4& a,u32x4& b,u32x4& c,u32x4& d){ asm volatile("s_waitcnt vmcnt(%4) lgkmcnt(0)":"+v"(a),"+v"(b),"+v"(c),"+v"(d):"n"(N):"memory"); }
DI void glds16(const char* sb, unsigned voff, unsigned ldsaddr){
  asm volatile("s_mov_b32 m0, %2\n\ts_nop 0\n\tglobal_load_lds_dwordx4 %0, %1"::"v"(voff),"s"(sb),"s"(ldsaddr):"memory"); }
DI void glds16q(const char* sb, unsigned voff, unsigned ldsaddr){
  asm volatile("s_mov_b32 m0, %2\n\ts_mov_b64 exec, 0xffff\n\tglobal_load_lds_dwordx4 %0, %1\n\ts_mov_b64 exec, -1"::"v"(voff),"s"(sb),"s"(ldsaddr):"memory"); }
DI void glds16v(const char* vp, unsigned ldsaddr){
  asm volatile("s_mov_b32 m0, %1\n\ts_nop 0\n\tglobal_load_lds_dwordx4 %0, off"::"v"(vp),"s"(ldsaddr):"memory"); }

#define IC(n) std::integral_constant<int,(n)>{}
#define SB() __builtin_amdgcn_sched_barrier(0)
DI void gld1(u32x4& a,unsigned voff,const char* sb){ asm volatile("global_load_dwordx4 %0, %1, %2":"=&v"(a):"v"(voff),"s"(sb):"memory"); }
DI unsigned opaque(unsigned v){ asm volatile("":"+v"(v)); return v; }
DI void pin5(u32x4& a,u32x4& b,u32x4& c,u32x4& d,u32x4& e){ asm volatile("":"+v"(a),"+v"(b),"+v"(c),"+v"(d),"+v"(e)); }
DI void pin9(unsigned (&c)[9]){ asm volatile("":"+v"(c[0]),"+v"(c[1]),"+v"(c[2]),"+v"(c[3]),"+v"(c[4]),"+v"(c[5]),"+v"(c[6]),"+v"(c[7]),"+v"(c[8])); }
template<int C> DI unsigned addc(unsigned v){ unsigned r; asm volatile("v_add_u32 %0, %1, %2":"=v"(r):"n"(C),"v"(v)); return r; }
DI float fma1(float a,float b,float c){ float r; asm("v_fma_f32 %0, %1, %2, %3":"=v"(r):"v"(a),"v"(b),"v"(c)); return r; }
DI float opf(float v){ asm("":"+v"(v)); return v; }
DI void gld_dw(unsigned& a,unsigned voff,const char* sb){ asm volatile("global_load_dword %0, %1, %2":"=&v"(a):"v"(voff),"s"(sb):"memory"); }
DI void gld_dw2(f32x2& a,unsigned voff,const char* sb){ asm volatile("global_load_dwordx2 %0, %1, %2":"=&v"(a):"v"(voff),"s"(sb):"memory"); }
DI void pin3(u32x4 (&c)[3]){ asm volatile("":"+v"(c[0]),"+v"(c[1]),"+v"(c[2])); }
DI void pin9f(f32x2 (&c)[9]){ asm volatile("":"+v"(c[0]),"+v"(c[1]),"+v"(c[2]),"+v"(c[3]),"+v"(c[4]),"+v"(c[5]),"+v"(c[6]),"+v"(c[7]),"+v"(c[8])); }

constexpr int NCNT=128;
__global__ __launch_bounds__(512) void k0(const u16* __restrict__ x,const u16* __restrict__ wqkv,const u16* __restrict__ wphi,const u16* __restrict__ wo,
                                          char* __restrict__ xf,char* __restrict__ wf,char* __restrict__ of,char* __restrict__ pf,unsigned* __restrict__ cnt){
  __shared__ __attribute__((aligned(16))) u16 sm[48*3*40+64];
  const int bx=blockIdx.x, tid=threadIdx.x, lane=tid&63, w=tid>>6;
  if(bx<32){
    const int tok=bx*64+w*8+(lane>>3), sub=lane&7;
    u32x4 v[9]; float s=0.f;
    #pragma unroll
    for(int i=0;i<9;i++) v[i]=*(const GAS u32x4*)((const char*)x+(size_t)tok*1152+(8*i+sub)*16);
    #pragma unroll
    for(int i=0;i<9;i++){
      #pragma unroll
      for(int e=0;e<4;e++) s+=lo16(v[i][e])+hi16(v[i][e]); }
    s+=__shfl_xor(s,1); s+=__shfl_xor(s,2); s+=__shfl_xor(s,4);
    const float m=s*(1.f/D); float q=0.f;
    #pragma unroll
    for(int i=0;i<9;i++){
      #pragma unroll
      for(int e=0;e<4;e++){ float a=lo16(v[i][e])-m, b=hi16(v[i][e])-m; q+=a*a+b*b; } }
    q+=__shfl_xor(q,1); q+=__shfl_xor(q,2); q+=__shfl_xor(q,4);
    const float r=__builtin_amdgcn_rsqf(q*(1.f/D)+1e-5f);
    #pragma unroll
    for(int i=0;i<9;i++){ u32x4 o;
      #pragma unroll
      for(int e=0;e<4;e++) o[e]=pk2((lo16(v[i][e])-m)*r,(hi16(v[i][e])-m)*r);
      const int c=8*i+sub;
      *(GAS u32x4*)(xf+(((size_t)(tok>>5)*36+(c>>1))*64+(c&1)*32+(tok&31))*16)=o; }
    if(bx==0 && tid<NCNT) cnt[tid*32]=0;
  } else if(bx<32+192){
    const int u=bx-32, hp=u/12, k0=(u%12)*48;
    for(int e=tid;e<48*3*18;e+=512){ const int row=e/54, c=e%54, part=c/18, d2=c%18;
      *(unsigned*)(sm+(row*3+part)*36+d2*2)=*(const unsigned*)(wqkv+(size_t)(k0+row)*1728+part*576+hp*36+d2*2); }
    __syncthreads();
    for(int e=tid;e<768;e+=512){ const int l=e&63, ks=(e>>6)%3, tile=(e>>6)/3, m=l&31, hk=l>>5;
      const int hd=(m>>2)&1, sg=16*(tile&1)+4*(m>>3)+(m&3), role=tile>>1;
      int part,d; bool ok=true;
      if(sg<18){ part=role; d=sg; } else { part=2; d=sg-18+14*role; ok=d<18; }
      const u16* p=sm+((ks*16+8*hk)*3+part)*36+hd*18+(ok?d:0);
      unsigned o[4];
      #pragma unroll
      for(int jj=0;jj<4;jj++) o[jj]=ok?((unsigned)p[(2*jj)*108]|((unsigned)p[(2*jj+1)*108]<<16)):0u;
      *(GAS u32x4*)(wf+(((size_t)(hp*4+tile)*36+k0/16+ks)*64+l)*16)=u32x4{o[0],o[1],o[2],o[3]}; }
    if(u%12==0 && tid<384){ const int l=tid&63, i=tid>>6, t=i/3, kp=i%3, m=l&31, hk=l>>5;
      const int f=16*t+4*(m>>3)+(m&3); const bool ok=(((m>>2)&1)==hk) && f<18;
      unsigned o[4];
      #pragma unroll
      for(int jj=0;jj<4;jj++){ const int d=8*kp+2*jj;
        const u16* p=wphi+((2*hp+hk)*18+d)*18+f;
        o[jj]=(ok&&d<18)?((unsigned)p[0]|((unsigned)p[18]<<16)):0u; }
      *(GAS u32x4*)(pf+((hp*6+i)*64+l)*16)=u32x4{o[0],o[1],o[2],o[3]}; }
  } else {
    const int u=bx-224;
    for(int fr=u*8+w; fr<648; fr+=256){ const int nt=fr/36, ks=fr%36, m=lane&31, hk=lane>>5;
      unsigned o[4];
      #pragma unroll
      for(int jj=0;jj<4;jj++){ const int idx=8*ks+2*jj, row=36*(idx/18)+18*hk+idx%18;
        const u16* p=wo+(size_t)row*576+nt*32+m; o[jj]=(unsigned)p[0]|((unsigned)p[576]<<16); }
      *(GAS u32x4*)(of+((size_t)fr*64+lane)*16)=u32x4{o[0],o[1],o[2],o[3]}; }
  }
}

constexpr int KVP=2*19*36*4;
DI void softmax2(const f32x16& p0,const f32x16& p1,float (&e1)[18],float (&e2)[18],float& s1,float& s2){
  float p[18];
  #pragma unroll
  for(int i=0;i<16;i++) p[i]=p0[i];
  p[16]=p1[0]; p[17]=p1[1];
  float mx=p[0],mn=p[0];
  #pragma unroll
  for(int i=1;i<18;i++){ mx=fmaxf(mx,p[i]); mn=fminf(mn,p[i]); }
  const float L=1.44269504f, a=-mx*L, b=mn*L; s1=0.f; s2=0.f;
  #pragma unroll
  for(int i=0;i<18;i++){ e1[i]=__builtin_amdgcn_exp2f(fmaf(p[i],L,a)); e2[i]=__builtin_amdgcn_exp2f(fmaf(p[i],-L,b)); s1+=e1[i]; s2+=e2[i]; }
}
DI bf16x8 pack8(const f32x16& c,int o){ u32x4 r={pk2(c[o],c[o+1]),pk2(c[o+2],c[o+3]),pk2(c[o+4],c[o+5]),pk2(c[o+6],c[o+7])}; return __builtin_bit_cast(bf16x8,r); }
__global__ __launch_bounds__(512,2) void k1(const char* __restrict__ xf,const char* __restrict__ wf,const char* __restrict__ pf,char* __restrict__ kvp,unsigned* __restrict__ cnt,char* __restrict__ ob){
  extern __shared__ __attribute__((aligned(16))) char lds_[];
  LAS char* lds=(LAS char*)lds_; const unsigned lb=(unsigned)(size_t)lds; STAMP_DECL STAMP(0);
  const int tid=threadIdx.x, lane=tid&63, w=__builtin_amdgcn_readfirstlane(tid>>6);
  const int bx=blockIdx.x, hp=2*(bx&7)+((bx>>3)&1), tk=bx>>4;
  const int tg=w&3, role=w>>2, hh=lane>>5, r=lane&31;
  const unsigned l16=lane*16;
  const char* aS=wf+((size_t)(hp*4+(w>>1))*36+(w&1)*2)*1024;
  const char* bS=xf+((size_t)(tk*4+(w>>1))*36+(w&1)*2)*1024;
  auto stage=[&](int kb,int pc){ const unsigned L=lb+(kb&3)*32768+w*2048+(pc&1)*1024+(pc>>1)*16384;
    glds16(((pc>>1)?bS:aS)+kb*4096+(pc&1)*1024,l16,L); };
  #pragma unroll
  for(int kb=0;kb<3;kb++){
    #pragma unroll
    for(int pc=0;pc<4;pc++) stage(kb,pc); }
  u32x4 pw[6];
  #pragma unroll
  for(int i=0;i<6;i++) gld1(pw[i],l16,pf+(hp*6+i)*1024);
  f32x16 c0={},c1={};
  { const int aA=role*8192+l16, aB=16384+tg*4096+l16;
    bf16x8 fa0[2],fa1[2],fb[2];
    auto rd=[&](int kb,int s,int b){ LAS char* L=lds+(kb&3)*32768+s*1024; fa0[b]=*(LAS bf16x8*)(L+aA); fa1[b]=*(LAS bf16x8*)(L+aA+4096); fb[b]=*(LAS bf16x8*)(L+aB); };
    auto mm=[&](int b){ c0=MFMA32(fa0[b],fb[b],c0); c1=MFMA32(fa1[b],fb[b],c1); SB(); };
    static_for<0,9>([&](auto KB){ constexpr int kb=decltype(KB)::value;
      wait_vm<(kb==0)?14:(kb==1)?10:(kb==8)?0:4>(); bar();
      if constexpr(kb==0) STAMP(1);
      constexpr bool ld=(kb>=1&&kb<=6);
      rd(kb,0,0); SB(); if constexpr(kb>0) mm(1); if constexpr(ld) stage(kb+2,0);
      rd(kb,1,1); SB(); mm(0); if constexpr(ld) stage(kb+2,1);
      rd(kb,2,0); SB(); mm(1); if constexpr(ld) stage(kb+2,2);
      rd(kb,3,1); SB(); mm(0); if constexpr(ld) stage(kb+2,3);
    });
    mm(1); }
  STAMP(2);
  pin3(*(u32x4(*)[3])&pw[0]); pin3(*(u32x4(*)[3])&pw[3]);
  LAS char* TI=lds+131072;
  const int tl=tg*32+r, sig=(((tl>>1)&1)<<2)|(tl&1)|(((tl>>2)&1)<<1);
  LAS char* trow=TI+(hh*128+tl)*128;
  f32x16 p0={},p1={};
  { const bf16x8 b0=pack8(c0,0), b1=pack8(c0,8), b2=pack8(c1,0);
    p0=MFMA32(__builtin_bit_cast(bf16x8,pw[0]),b0,p0); p1=MFMA32(__builtin_bit_cast(bf16x8,pw[3]),b0,p1);
    p0=MFMA32(__builtin_bit_cast(bf16x8,pw[1]),b1,p0); p1=MFMA32(__builtin_bit_cast(bf16x8,pw[4]),b1,p1);
    p0=MFMA32(__builtin_bit_cast(bf16x8,pw[2]),b2,p0); p1=MFMA32(__builtin_bit_cast(bf16x8,pw[5]),b2,p1); }
  if(role==0){
    *(LAS u32x4*)(trow+((5^sig)*16))=u32x4{pk2(c1[2],c1[3]),pk2(c1[4],c1[5]),pk2(c1[6],c1[7]),pk2(c1[8],c1[9])};
    *(LAS u32x2*)(trow+((6^sig)*16))=u32x2{pk2(c1[10],c1[11]),pk2(c1[12],c1[13])};
    *(LAS unsigned*)(trow+((6^sig)*16)+8)=pk2(c1[14],c1[15]);
  } else {
    *(LAS unsigned*)(trow+((6^sig)*16)+12)=pk2(c1[2],c1[3]);
    *(LAS u32x4*)(trow+((7^sig)*16))=u32x4{pk2(c1[4],c1[5]),0x00003f80u,0u,0u};
  }
  float e1[18],e2[18],s1,s2;
  softmax2(p0,p1,e1,e2,s1,s2);
  unsigned ph[20];
  { const float i2=__builtin_amdgcn_rcpf(s2);
    const float a1=role?__builtin_amdgcn_rcpf(s1):1.f, a2=role?i2:s1*i2;
    #pragma unroll
    for(int i=0;i<9;i++){ ph[i]=pk2(e1[2*i]*a1,e1[2*i+1]*a1); ph[9+i]=pk2(e2[2*i]*a2,e2[2*i+1]*a2); }
    ph[18]=0u; ph[19]=0u; }
  if(role==1){
    #pragma unroll
    for(int i=0;i<5;i++) *(LAS u32x4*)(trow+((i^sig)*16))=u32x4{ph[4*i],ph[4*i+1],ph[4*i+2],ph[4*i+3]};
  }
  STAMP(3);
  wait_lgkm0(); bar();
  const __amdgpu_buffer_rsrc_t rs=__builtin_amdgcn_make_buffer_rsrc(kvp,0,256*KVP,0x00020000);
  if(w<4){
    const int q=(lane&15)>>2, p=lane&3, g2=(lane>>4)&1, hd=w&1, ft=w>>1;
    f32x16 kc={};
    #pragma unroll
    for(int s=0;s<8;s++){
      s16x4 a[2],b[2];
      #pragma unroll
      for(int t=0;t<2;t++){ const int tk2=16*s+8*hh+4*t+q, sg=((q>>1)<<2)|(q&1)|(t<<1);
        LAS char* rp=TI+(hd*128+tk2)*128+(p&1)*8;
        a[t]=__builtin_amdgcn_ds_read_tr16_b64_v4i16((LAS s16x4*)(rp+(((4*ft+2*g2+(p>>1))^sg)*16)));
        b[t]=__builtin_amdgcn_ds_read_tr16_b64_v4i16((LAS s16x4*)(rp+((((5+2*g2+(p>>1))&7)^sg)*16))); }
      kc=MFMA32(__builtin_bit_cast(bf16x8,__builtin_shufflevector(a[0],a[1],0,1,2,3,4,5,6,7)),__builtin_bit_cast(bf16x8,__builtin_shufflevector(b[0],b[1],0,1,2,3,4,5,6,7)),kc);
    }
    const unsigned base=(unsigned)((hp*16+tk)*KVP+(hd*19+r)*144+hh*16);
    if(r<19){
      if(ft==0){
        #pragma unroll
        for(int i=0;i<4;i++) __builtin_amdgcn_raw_buffer_store_b128(u32x4{__float_as_uint(kc[4*i]),__float_as_uint(kc[4*i+1]),__float_as_uint(kc[4*i+2]),__float_as_uint(kc[4*i+3])},rs,base+i*32,0,16);
      } else if(hh==0) __builtin_amdgcn_raw_buffer_store_b128(u32x4{__float_as_uint(kc[0]),__float_as_uint(kc[1]),__float_as_uint(kc[2]),__float_as_uint(kc[3])},rs,base+128,0,16);
    }
  }
  { LAS u32x4* z=(LAS u32x4*)lds; z[tid]=u32x4{0u,0u,0u,0u}; if(tid<128) z[512+tid]=u32x4{0u,0u,0u,0u}; }
  asm volatile("s_waitcnt vmcnt(0) lgkmcnt(0)":::"memory");
  STAMP(4);
  bar();
  GAS unsigned* cn=(GAS unsigned*)cnt+(64+hp)*32;
  if(tid==0) __hip_atomic_fetch_add(cn,1u,RLX_AGENT);
  if(w==0){
    for(unsigned spins=0;spins<(1u<<20);spins++){
      if((unsigned)__builtin_amdgcn_readfirstlane(__hip_atomic_load(cn,RLX_AGENT))>=16u) break;
      __builtin_amdgcn_s_sleep(1);
    }
  }
  bar();
  STAMP(5);
  if(tid<342){
    u32x4 pv[16];
    #pragma unroll
    for(int i=0;i<16;i++) pv[i]=__builtin_amdgcn_raw_buffer_load_b128(rs,(unsigned)((hp*16+i)*KVP+tid*16),0,16);
    float a[4]={0.f,0.f,0.f,0.f};
    #pragma unroll
    for(int i=0;i<16;i++){
      #pragma unroll
      for(int e=0;e<4;e++) a[e]+=__uint_as_float(pv[i][e]); }
    const int hd=tid/171, rem=tid-hd*171, rho=rem/9, f4=rem-rho*9;
    const int t=rho>>4, ph_=rho&15, m=8*(ph_>>2)+4*hd+(ph_&3);
    *(LAS u32x2*)(lds+((t*5+(f4>>1))*64+hd*32+m)*16+(f4&1)*8)=u32x2{pk2(a[0],a[1]),pk2(a[2],a[3])};
  }
  wait_lgkm0(); bar();
  STAMP(6);
  if(role==0){
    f32x16 o0={},o1={};
    #pragma unroll
    for(int kp=0;kp<5;kp++){ const bf16x8 b=__builtin_bit_cast(bf16x8,u32x4{ph[4*kp],ph[4*kp+1],ph[4*kp+2],ph[4*kp+3]});
      o0=MFMA32(*(LAS bf16x8*)(lds+kp*1024+l16),b,o0); o1=MFMA32(*(LAS bf16x8*)(lds+5120+kp*1024+l16),b,o1); }
    const float id=__builtin_amdgcn_rcpf(fmaf(s1,1e-6f,o1[2]));
    char* dst=ob+(size_t)(tk*4+tg)*36*1024+l16;
    #pragma unroll
    for(int i=0;i<9;i++){ const unsigned v=(i<8)?pk2(o0[2*i]*id,o0[2*i+1]*id):pk2(o1[0]*id,o1[1]*id);
      const int B=36*hp+4*i; *(GAS unsigned*)(dst+(B>>4)*1024+(B&15))=v; }
  }
  STAMP(7); STAMP_OUT(2);
}

__global__ __launch_bounds__(512,2) void k2(const char* __restrict__ ob,const char* __restrict__ of,const u16* __restrict__ x,char* __restrict__ hf,char* __restrict__ stats,
                                            const u16* __restrict__ wg,const u16* __restrict__ wu,unsigned* __restrict__ cs){
  extern __shared__ __attribute__((aligned(16))) char lds_[];
  LAS char* lds=(LAS char*)lds_; const unsigned lb=(unsigned)(size_t)lds; STAMP_DECL STAMP(0);
  const int tid=threadIdx.x, lane=tid&63, w=__builtin_amdgcn_readfirstlane(tid>>6);
  const int bx=blockIdx.x; const unsigned l16=lane*16;
  if(bx<144){
    const int tk=(bx&7)*2+((bx>>3)&1), np=bx>>4;
    const int tg=w&3, nt=w>>2, hh=lane>>5, r=lane&31;
    const char* aS=of+((size_t)(np*2+(w>>2))*36+(w&3))*1024;
    const char* bS=ob+((size_t)(tk*4+(w>>1))*36+(w&1)*2)*1024;
    auto stage=[&](int kb){ const unsigned L=lb+(kb%6)*24576;
      glds16(aS+kb*4096,l16,L+w*1024); glds16(bS+kb*4096,l16,L+8192+w*2048); glds16(bS+kb*4096+1024,l16,L+8192+w*2048+1024); };
    #pragma unroll
    for(int kb=0;kb<5;kb++) stage(kb);
    f32x2 xr[4];
    { const char* xs=(const char*)x+((size_t)(tk*128+tg*32)*576+np*64+nt*32)*2; const unsigned xo=r*1152+hh*8;
      #pragma unroll
      for(int i=0;i<4;i++) gld_dw2(xr[i],xo,xs+i*16); }
    f32x16 c={};
    const int aA=nt*4096+l16, aB=8192+tg*4096+l16;
    static_for<0,9>([&](auto KB){ constexpr int kb=decltype(KB)::value;
      constexpr int n=(kb==0)?16:(kb<=4)?(4-kb)*3+4+((kb-1<4?kb-1:4))*3:(8-kb)*3;
      wait_vm<n>(); bar();
      if constexpr(kb==0) STAMP(1);
      if constexpr(kb>=1&&kb<=4) stage(kb+4);
      LAS char* L=lds+(kb%6)*24576;
      bf16x8 a[4],b[4];
      #pragma unroll
      for(int s=0;s<4;s++){ a[s]=*(LAS bf16x8*)(L+aA+s*1024); b[s]=*(LAS bf16x8*)(L+aB+s*1024); }
      #pragma unroll
      for(int s=0;s<4;s++) c=MFMA32(a[s],b[s],c);
      wait_lgkm0();
    });
    STAMP(2);
    asm volatile("":"+v"(xr[0]),"+v"(xr[1]),"+v"(xr[2]),"+v"(xr[3]));
    unsigned o[8]; float v[16], sm=0.f;
    #pragma unroll
    for(int i=0;i<4;i++){ const unsigned x0=__float_as_uint(xr[i][0]), x1=__float_as_uint(xr[i][1]);
      o[2*i]=pk2(c[4*i]+lo16(x0),c[4*i+1]+hi16(x0)); o[2*i+1]=pk2(c[4*i+2]+lo16(x1),c[4*i+3]+hi16(x1)); }
    char* dst=hf+((size_t)(tk*4+tg)*36+np*4+nt*2)*1024+l16;
    *(GAS u32x4*)dst=u32x4{o[0],o[1],o[2],o[3]}; *(GAS u32x4*)(dst+1024)=u32x4{o[4],o[5],o[6],o[7]};
    #pragma unroll
    for(int i=0;i<8;i++){ v[2*i]=lo16(o[i]); v[2*i+1]=hi16(o[i]); sm+=v[2*i]+v[2*i+1]; }
    sm=(sm+__shfl_xor(sm,32))*(1.f/32);
    float q=0.f;
    #pragma unroll
    for(int i=0;i<16;i++){ const float d=v[i]-sm; q+=d*d; }
    q+=__shfl_xor(q,32);
    if(hh==0) *(GAS f32x2*)(stats+((size_t)np*S+tk*128+tg*32+r)*16+nt*8)=f32x2{sm,q};
    STAMP(3);
  } else if(bx<144+72){
    const int u=bx-144, m=u/36, cb=u%36;
    const char* src=(const char*)(m?wu:wg)+cb*256+(size_t)(w*72+(lane>>4))*(F*2)+(lane&15)*16;
    float a[8]={0.f,0.f,0.f,0.f,0.f,0.f,0.f,0.f};
    u32x4 v[18];
    #pragma unroll
    for(int i=0;i<18;i++) v[i]=*(const GAS u32x4*)(src+(size_t)i*4*(F*2));
    #pragma unroll
    for(int i=0;i<18;i++){
      #pragma unroll
      for(int e=0;e<4;e++){ a[2*e]+=lo16(v[i][e]); a[2*e+1]+=hi16(v[i][e]); } }
    #pragma unroll
    for(int e=0;e<8;e++){ a[e]+=__shfl_xor(a[e],16); a[e]+=__shfl_xor(a[e],32); }
    LAS float* red=(LAS float*)lds;
    if(lane<16){
      #pragma unroll
      for(int e=0;e<8;e++) red[w*128+lane*8+e]=a[e]; }
    __syncthreads();
    if(tid<128){ float s=0.f;
      #pragma unroll
      for(int i=0;i<8;i++) s+=red[i*128+tid];
      const unsigned hi=pk2(s,s)&0xffffu, lo=pk2(s-lo16(hi),0.f)&0xffffu;
      const int f=cb*128+tid; int j,t,l;
      if(f<4096){ j=f>>7; t=((f>>5)&3)*2+m; l=f&31; } else { j=(f-4096)>>4; t=8; l=(f&15)+16*m; }
      cs[((j*3+(t>>2))*32+l)*4+(t&3)]=hi|(lo<<16); }
  }
  STAMP_OUT(3);
}

__global__ __launch_bounds__(512,2) void k3(const char* __restrict__ hf,const char* __restrict__ stats,const char* __restrict__ cs,const u16* __restrict__ wg,const u16* __restrict__ wu,char* __restrict__ gf){
  extern __shared__ __attribute__((aligned(16))) char lds_[];
  LAS char* lds=(LAS char*)lds_; const unsigned lb=(unsigned)(size_t)lds; STAMP_DECL STAMP(0);
  const int lane=threadIdx.x&63, w=__builtin_amdgcn_readfirstlane(threadIdx.x>>6);
  const int bx=blockIdx.x, xcd=bx&7, ii=bx>>3;
  const int tt=4*(xcd&1)+(ii&3), j=8*(xcd>>1)+(ii>>2);
  const int T=tt*8+w;
  const unsigned voffS=(unsigned)((8*w+(lane>>3))*(F*2)+(((lane&7)^(((lane>>4)&1)<<2))*16));
  const char* gA=(const char*)wg+j*256; const char* uA=(const char*)wu+j*256;
  const unsigned xoff=lane*16; const char* xb=hf+(size_t)T*36*1024;
  u32x4 x[36], sp[5], cv[3];
  auto ldw=[&](int m,int kb,int ch,int ps){ glds16((m?uA:gA)+ch*128+(size_t)kb*64*F*2, voffS, lb+ps*16384+m*8192+w*1024); };
  ldw(0,0,0,0); ldw(1,0,0,0); gld4x4(x[0],x[1],x[2],x[3],xoff,xb);
  ldw(0,1,0,1); ldw(1,1,0,1); gld4x4(x[4],x[5],x[6],x[7],xoff,xb+4096);
  { const unsigned l16=(lane&31)*16, so=l16+(lane>>5)*(4*S*16); const char* sb=stats+(size_t)T*32*16;
    gld1(sp[0],so,sb); gld1(sp[1],so,sb+S*16); gld1(sp[2],so,sb+2*S*16); gld1(sp[3],so,sb+3*S*16); gld1(sp[4],l16,sb+8*S*16);
    gld1(cv[0],l16,cs+j*1536); gld1(cv[1],l16,cs+j*1536+512); gld1(cv[2],l16,cs+j*1536+1024); }
  ldw(0,2,0,2); ldw(1,2,0,2); gld4x4(x[8],x[9],x[10],x[11],xoff,xb+8192);
  if(w>=4) __builtin_amdgcn_s_setprio(1);
  const int q=(lane&15)>>2, p=lane&3, g2=(lane>>4)&1, hh=lane>>5;
  const unsigned a0_=(4*hh+q)*128+(((2*g2+(p>>1))^((q>>1)<<2))*16)+(p&1)*8, a0=lb+a0_, a1=lb+(a0_^64);
  const unsigned ac=lb+(4*hh+q)*64+(2*g2+(p>>1))*16+(p&1)*8;
  auto ldc=[&](int pi,int sl){ unsigned l4=opaque(xoff)>>4; const int row0=pi*128+16*w-((pi==4&&w>=4)?64:0);
    const char* m=(const char*)((l4&2)?wu:wg)+(size_t)(4096+16*j)*2+(size_t)row0*(F*2);
    glds16v(m+((l4>>2)*(F*2)+(l4&1)*16), lb+sl*8192+w*1024); };
  f32x16 acc[4]={};
  char* outb=gf+(size_t)T*288*1024;
  #define XB(s) __builtin_bit_cast(bf16x8,x[s])
  constexpr int PD=4, RING=4, NU=324;
  bf16x8 A[RING]; u32x4 eo, bm; float k1, iv, sm;
  auto unit=[](int u,int& kb,int& s,int& t){
    if(u<96){ kb=u>>4; s=(u>>2)&3; t=u&3; }
    else if(u<120){ int v=u-96; kb=6+(v>>3); s=(v>>1)&3; t=v&1; }
    else if(u<144){ int v=u-120; kb=6+(v>>3); s=(v>>1)&3; t=2+(v&1); }
    else if(u<216){ int v=u-144; kb=v>>3; s=(v>>1)&3; t=v&1; }
    else if(u<288){ int v=u-216; kb=v>>3; s=(v>>1)&3; t=2+(v&1); }
    else { int v=u-288; kb=v>>2; s=v&3; t=0; } };
  unsigned pb0=0,pb1=0;
  auto rd=[&](auto U){ constexpr int u=decltype(U)::value; int kb=0,s=0,t=0; unit(u,kb,s,t);
    if constexpr(u<288){ constexpr int kbc=(u<96)?(u>>4):(u<120)?6+((u-96)>>3):(u<144)?6+((u-120)>>3):(u<216)?((u-144)>>3):((u-216)>>3); constexpr int ps=(u<144)?kbc:(kbc+9)%10;
      if constexpr(u<96){ if constexpr((u&15)==0) pb0=addc<ps*16384>(a0); if constexpr((u&15)==2) pb1=addc<ps*16384>(a1); }
      else if constexpr((u&7)==0){ if constexpr(u<120||(u>=144&&u<216)) pb0=addc<ps*16384>(a0); else pb1=addc<ps*16384>(a1); }
      A[u%RING]=ldtr<1024>((LAS char*)(size_t)(((t&2)?pb1:pb0))+(t&1)*8192+s*2048); }
    else { constexpr int v=u-288, pi=v/8, sl=(pi<4)?16+pi:0;
      if constexpr((v&7)==0) pb0=addc<sl*8192>(ac);
      A[u%RING]=ldtr<512>((LAS char*)(size_t)pb0+(v%8)*1024); } };
  float ea[2], eb[2], ep;
  auto job=[&](auto I,auto NE,const f32x16& g,const f32x16& u,int uo,int fs){ constexpr int i=decltype(I)::value, ne=decltype(NE)::value;
    constexpr int k=i/3, st=i%3, e=k-st;
    if constexpr(e>=0 && e<ne){
      if constexpr(st==0) ea[e&1]=__builtin_amdgcn_exp2f(g[e]*k1);
      if constexpr(st==1) eb[e&1]=__builtin_amdgcn_rcpf(fmaf(ea[e&1],iv,iv));
      if constexpr(st==2){ float v=opf(g[e]*u[uo+e]*eb[e&1]);
        if constexpr((e&1)==0) ep=v; else eo[(e>>1)&3]=pk2(ep,v);
        if constexpr((e&7)==7) *(GAS u32x4*)(outb+(fs+(e>>3))*1024+xoff)=eo; } } };
  auto spread=[&](auto U,auto U0,auto L,const f32x16& g,const f32x16& uu,int fs){ constexpr int u=decltype(U)::value,u0=decltype(U0)::value,l=decltype(L)::value;
    if constexpr(u>=u0 && u<u0+l){ constexpr int b=(u-u0)*54/l, e=(u-u0+1)*54/l; static_for<b,e>([&](auto I){ job(I,IC(16),g,uu,0,fs); }); } };
  auto corr=[&](unsigned d,const f32x16& c){ u32x4 a={d,d&0xffffu,0u,0u}; return MFMA32(__builtin_bit_cast(bf16x8,a),__builtin_bit_cast(bf16x8,bm),c); };
  static_for<-PD,NU>([&](auto U){ constexpr int u=decltype(U)::value; constexpr int r=u+PD;
    if constexpr(r<=96 && (r&15)==0){ constexpr int kb=r>>4;
      constexpr int n=(kb==0||kb==1)?20:12;
      wait_vm4n<n>(x[4*kb],x[4*kb+1],x[4*kb+2],x[4*kb+3]);
      bar();
      if constexpr(kb==0) STAMP(2); if constexpr(kb==2) STAMP(3); if constexpr(kb==4) STAMP(4); if constexpr(kb==6) STAMP(5); }
    if constexpr(r==104){ wait_vm4n<9>(x[28],x[29],x[30],x[31]); bar(); }
    if constexpr(r==112){ wait_vm4n<6>(x[32],x[33],x[34],x[35]); bar(); STAMP(6); }
    if constexpr(r==144){ wait_vm<3>(); bar(); STAMP(7); }
    if constexpr(r==144+40){ wait_vm<2>(); bar(); STAMP(8); }
    if constexpr(r==288){ wait_vm<0>(); bar(); STAMP(10); }
    if constexpr(u>=0){ int kb=0,s=0,t=0; unit(u,kb,s,t);
      if constexpr(u==144||u==145) acc[u-144]=corr(cv[1][u-144],f32x16{});
      if constexpr(u==216||u==217) acc[u-214]=corr(cv[1][2+u-216],f32x16{});
      if constexpr(u==288) acc[0]=corr(cv[2][0],f32x16{});
      if constexpr(u>=56&&u<60) acc[u-56]=corr(cv[0][u-56],acc[u-56]);
      acc[t]=MFMA32(A[u%RING],XB(u<288?4*kb+s:u-288),acc[t]);
      SB(); }
    if constexpr(r<NU) rd(IC(r));
    if constexpr(r<96){ constexpr int kb=r>>4, o=r&15; constexpr int slot=(o==1)?0:(o==3)?1:(o==6)?2:(o==8)?3:(o==11)?4:(o==13)?5:-1;
      if constexpr(slot>=0){ if constexpr(slot<2) ldw(slot,kb+3,0,kb+3); else gld1(x[4*(kb+3)+slot-2],xoff,xb+(4*(kb+3)+slot-2)*1024); } }
    else if constexpr(r<128){ constexpr int o=(r-96)&7; constexpr int slot=(o==1)?0:(o==3)?1:(o==6)?2:-1;
      if constexpr(slot>=0){ constexpr int n=((r-96)>>3)*3+slot, i=n>>1; ldw(n&1,i,1,i==0?9:i-1); } }
    if constexpr(r==130) ldw(0,6,1,5);
    if constexpr(r==133) ldw(1,6,1,5);
    if constexpr(r>=36 && r<48){
      if constexpr(r==36){ pin5(sp[0],sp[1],sp[2],sp[3],sp[4]); pin3(cv); }
      if constexpr(r==37){ float a=__uint_as_float(hh?sp[4][2]:sp[4][0]);
        #pragma unroll
        for(int i=0;i<4;i++) a+=__uint_as_float(sp[i][0])+__uint_as_float(sp[i][2]);
        sm=(a+__shfl_xor(a,32))*(1.f/18); }
      if constexpr(r==41){ float d=__uint_as_float(hh?sp[4][2]:sp[4][0])-sm; float a=__uint_as_float(hh?sp[4][3]:sp[4][1]), b=d*d;
        #pragma unroll
        for(int i=0;i<8;i++){ d=__uint_as_float(sp[i>>1][(i&1)*2])-sm; a+=__uint_as_float(sp[i>>1][(i&1)*2+1]); b=fmaf(d,d,b); }
        a=fmaf(b,32.f,a);
        iv=(a+__shfl_xor(a,32))*(1.f/D)+1e-5f; }
      if constexpr(r==46){ k1=__builtin_amdgcn_rsqf(iv)*-1.44269504f;
        const unsigned mh=pk2(-sm,-sm); const float ml=-sm-lo16(mh);
        bm=u32x4{hh?0u:mh, hh?0u:(pk2(ml,ml)&0xffffu), 0u, 0u}; } }
    if constexpr(r==144+1) ldw(0,7,1,6);
    if constexpr(r==144+4) ldw(1,7,1,6);
    if constexpr(r==144+7) ldw(0,8,1,7);
    if constexpr(r==144+10) ldw(1,8,1,7);
    if constexpr(r==144+42) ldc(0,16);
    if constexpr(r==144+46) ldc(1,17);
    if constexpr(u==216+15){ SB(); bar(); STAMP(9); }
    if constexpr(u==216+16) ldc(2,18);
    if constexpr(u==216+19) ldc(3,19);
    if constexpr(u==216+22) ldc(4,0);
    spread(U,IC(121),IC(23),acc[0],acc[1],8*j);
    spread(U,IC(146),IC(34),acc[2],acc[3],8*j+2);
    spread(U,IC(218),IC(66),acc[0],acc[1],8*j+4);
    spread(U,IC(290),IC(34),acc[2],acc[3],8*j+6);
    SB();
  });
  static_for<0,30>([&](auto I){ job(I,IC(8),acc[0],acc[0],8,256+j); });
  STAMP(11);
}

constexpr int K4_STAGE=34816;
__global__ __launch_bounds__(512,2) void k4(const char* __restrict__ gf,const u16* __restrict__ wd,const char* __restrict__ hf,char* __restrict__ xch,unsigned* __restrict__ cnt,u16* __restrict__ outp){
  extern __shared__ __attribute__((aligned(16))) char lds_[];
  LAS char* lds=(LAS char*)lds_; const unsigned lb=(unsigned)(size_t)lds; STAMP_DECL STAMP(0);
  const int lane=threadIdx.x&63, w=__builtin_amdgcn_readfirstlane(threadIdx.x>>6);
  const int bx=blockIdx.x, xcd=bx&7, ii=bx>>3;
  const int sk=xcd>>1, tt=8*(xcd&1)+(ii&7), ng=ii>>3;
  const int tg=w&3, nh=w>>2;
  const unsigned voffW=(unsigned)((8*w+(lane>>3))*(D*2)+(((lane&7)^(((lane>>4)&1)<<2))*16));
  const unsigned voffQ=(unsigned)((8*w+((lane&15)>>1))*(D*2)+(lane&1)*16+1024+ng*32);
  const char* wb=(const char*)wd+ng*256;
  const char* hb=gf+((size_t)(tt*4+(w>>1))*288+(w&1)*2)*1024;
  auto stage=[&](int kb,int pc){
    int f0=(kb<16)?1024*sk+64*kb:4096+128*sk+64*(kb-16);
    const unsigned L=lb+(kb&3)*K4_STAGE;
    const char* ws=wb+(size_t)f0*D*2;
    const char* hs=hb+(size_t)(f0>>4)*1024;
    if(pc==0) glds16(ws, voffW, L+w*1024);
    if(pc==1) glds16(ws+128, voffW, L+8192+w*1024);
    if(pc==2) glds16(hs, lane*16, L+16384+w*2048);
    if(pc==3) glds16(hs+1024, lane*16, L+16384+w*2048+1024);
    if(pc==4) glds16q((const char*)wd+(size_t)f0*D*2, voffQ, L+32768+w*256);
  };
  #pragma unroll
  for(int kb=0;kb<3;kb++){
    #pragma unroll
    for(int pc=0;pc<5;pc++) stage(kb,pc); }
  const int q=(lane&15)>>2, p=lane&3, g2=(lane>>4)&1, hh=lane>>5;
  const int a0=nh*8192+(4*hh+q)*128+(((2*g2+(p>>1))^((q>>1)<<2))*16)+(p&1)*8, a1=a0^64;
  const int aq=32768+(4*hh+q)*32+(p>>1)*16+(p&1)*8;
  const int ah=16384+tg*4096+lane*16;
  f32x16 c0={},c1={},c2={};
  auto mainloop=[&](auto NH){ constexpr int nhc=decltype(NH)::value;
    bf16x8 fb[2],f0[2],f1[2],f2[2];
    auto rd=[&](LAS char* L,int s,int b){
      fb[b]=*(LAS bf16x8*)(L+ah+s*1024); f0[b]=ldtr<1024>(L+a0+s*2048); f1[b]=ldtr<1024>(L+a1+s*2048);
      if constexpr(nhc==0) f2[b]=ldtr<256>(L+aq+s*512); };
    auto mm=[&](int b,int kb,int pa,int pb){
      c0=MFMA32(f0[b],fb[b],c0); SB(); if(pa>=0) stage(kb,pa);
      c1=MFMA32(f1[b],fb[b],c1); SB(); if(pb>=0) stage(kb,pb);
      if constexpr(nhc==0) c2=MFMA32(f2[b],fb[b],c2);
      SB(); };
    auto step=[&](int kb,auto FIRST,auto LD){ constexpr bool first=decltype(FIRST)::value, ld=decltype(LD)::value;
      LAS char* L=lds+(kb&3)*K4_STAGE;
      rd(L,0,0); SB(); if constexpr(!first) mm(1,kb+2,ld?0:-1,-1);
      rd(L,1,1); SB(); mm(0,kb+2,(ld&&first)?0:-1,ld?1:-1);
      rd(L,2,0); SB(); mm(1,kb+2,ld?2:-1,ld?3:-1);
      rd(L,3,1); SB(); mm(0,kb+2,ld?4:-1,-1); };
    wait_vm<10>(); bar(); STAMP(1);
    step(0,IC(1),IC(0));
    for(int kb=1;kb<16;kb++){
      wait_vm<5>(); bar();
      if(kb==4) STAMP(2); if(kb==8) STAMP(3); if(kb==12) STAMP(4);
      step(kb,IC(0),IC(1));
    }
    wait_vm<5>(); bar(); step(16,IC(0),IC(0));
    wait_vm<0>(); bar(); step(17,IC(0),IC(0));
    mm(1,0,-1,-1);
  };
  if(nh==0) mainloop(IC(0)); else mainloop(IC(1));
  STAMP(5);
  const int tile=tt*4+ng;
  const __amdgpu_buffer_rsrc_t rs=__builtin_amdgcn_make_buffer_rsrc(xch,0,64*16*9*1024,0x00020000);
  GAS unsigned* cn=(GAS unsigned*)cnt+tile*32;
  if(tg!=sk){
    unsigned base=(unsigned)(((tile*4+tg)*4+sk)*9*1024+nh*4096+lane*16);
    u32x4 v;
    #pragma unroll
    for(int i=0;i<4;i++) v[i]=pk2(c0[2*i],c0[2*i+1]);
    __builtin_amdgcn_raw_buffer_store_b128(v,rs,base,0,16);
    #pragma unroll
    for(int i=0;i<4;i++) v[i]=pk2(c0[8+2*i],c0[8+2*i+1]);
    __builtin_amdgcn_raw_buffer_store_b128(v,rs,base+1024,0,16);
    #pragma unroll
    for(int i=0;i<4;i++) v[i]=pk2(c1[2*i],c1[2*i+1]);
    __builtin_amdgcn_raw_buffer_store_b128(v,rs,base+2048,0,16);
    #pragma unroll
    for(int i=0;i<4;i++) v[i]=pk2(c1[8+2*i],c1[8+2*i+1]);
    __builtin_amdgcn_raw_buffer_store_b128(v,rs,base+3072,0,16);
    if(nh==0){
      #pragma unroll
      for(int i=0;i<4;i++) v[i]=pk2(c2[2*i],c2[2*i+1]);
      __builtin_amdgcn_raw_buffer_store_b128(v,rs,base+8192,0,16);
    }
  }
  asm volatile("s_waitcnt vmcnt(0)":::"memory");
  STAMP(6);
  bar();
  if(threadIdx.x==0) __hip_atomic_fetch_add(cn,1u,RLX_AGENT);
  if(tg==sk){
    const int T=tt*4+tg;
    const char* hr=hf+((size_t)T*36+ng*8+nh*4)*1024+lane*16;
    u32x4 r[5];
    #pragma unroll
    for(int i=0;i<4;i++) r[i]=*(const GAS u32x4*)(hr+i*1024);
    r[4]=*(const GAS u32x4*)(hf+((size_t)T*36+32+ng)*1024+lane*16);
    for(unsigned spins=0;spins<(1u<<20);spins++){
      if((unsigned)__builtin_amdgcn_readfirstlane(__hip_atomic_load(cn,RLX_AGENT))>=4u) break;
      __builtin_amdgcn_s_sleep(1);
    }
    STAMP(7);
    __builtin_amdgcn_fence(__ATOMIC_ACQUIRE,"wavefront");
    u32x4 pv[3][5];
    #pragma unroll
    for(int s=0;s<3;s++){ int src=s+(s>=sk?1:0);
      unsigned base=(unsigned)(((tile*4+tg)*4+src)*9*1024+nh*4096+lane*16);
      #pragma unroll
      for(int i=0;i<4;i++) pv[s][i]=__builtin_amdgcn_raw_buffer_load_b128(rs,base+i*1024,0,16);
      pv[s][4]=__builtin_amdgcn_raw_buffer_load_b128(rs,base-nh*4096+8192,0,16);
    }
    u16* orow=outp+(size_t)(T*32+(lane&31))*D+4*hh;
    auto fin=[&](const f32x16& c,int half,int fi,int n0){
      float v[8];
      #pragma unroll
      for(int e=0;e<4;e++){
        v[2*e]=c[8*half+2*e]+lo16(r[fi][e])+lo16(pv[0][fi][e])+lo16(pv[1][fi][e])+lo16(pv[2][fi][e]);
        v[2*e+1]=c[8*half+2*e+1]+hi16(r[fi][e])+hi16(pv[0][fi][e])+hi16(pv[1][fi][e])+hi16(pv[2][fi][e]);
      }
      u32x2 a={pk2(v[0],v[1]),pk2(v[2],v[3])}, b={pk2(v[4],v[5]),pk2(v[6],v[7])};
      *(GAS u32x2*)(orow+n0)=a; *(GAS u32x2*)(orow+n0+8)=b;
    };
    const int nb=ng*128+nh*64;
    fin(c0,0,0,nb); fin(c0,1,1,nb+16); fin(c1,0,2,nb+32); fin(c1,1,3,nb+48);
    if(nh==0) fin(c2,0,4,512+ng*16);
    STAMP(8); asm volatile("s_waitcnt vmcnt(0)":::"memory"); STAMP(9);
  }
  STAMP_OUT(1);
}

extern "C" void kernel_launch(void* const* d_in, const int* in_sizes, int n_in, void* d_out, int out_size, void* d_ws, size_t ws_size, hipStream_t stream){
  const u16* x=(const u16*)d_in[0]; const u16* wqkv=(const u16*)d_in[1]; const u16* wphi=(const u16*)d_in[2]; const u16* wo=(const u16*)d_in[3];
  const u16* wg=(const u16*)d_in[5]; const u16* wu=(const u16*)d_in[6]; const u16* wd=(const u16*)d_in[7];
  char* ws=(char*)d_ws; size_t off=0; auto al=[&](size_t n){ char* p=ws+off; off+=(n+4095)&~4095ull; return p; };
  unsigned* cnt=(unsigned*)al(NCNT*128);
  char* xf=al(S*D*2); char* wf=al(16*4*36*1024); char* of=al(18*36*1024); char* pf=al(16*6*1024); char* kvp=al(256*KVP); char* ob=al(S*D*2);
  char* hf=al(S*D*2); char* stats=al(9*S*16); unsigned* cs=(unsigned*)al(32*3*32*16); char* gf=al((size_t)S*F*2); char* xch=al(64*16*9*1024);
  (void)hipFuncSetAttribute((const void*)k1,hipFuncAttributeMaxDynamicSharedMemorySize,163840);
  (void)hipFuncSetAttribute((const void*)k2,hipFuncAttributeMaxDynamicSharedMemorySize,147456);
  (void)hipFuncSetAttribute((const void*)k3,hipFuncAttributeMaxDynamicSharedMemorySize,163840);
  (void)hipFuncSetAttribute((const void*)k4,hipFuncAttributeMaxDynamicSharedMemorySize,4*K4_STAGE);
  k0<<<256,512,0,stream>>>(x,wqkv,wphi,wo,xf,wf,of,pf,cnt);
  k1<<<256,512,163840,stream>>>(xf,wf,pf,kvp,cnt,ob);
  k2<<<216,512,147456,stream>>>(ob,of,x,hf,stats,wg,wu,cs);
  k3<<<256,512,163840,stream>>>(hf,stats,(const char*)cs,wg,wu,gf);
  k4<<<256,512,4*K4_STAGE,stream>>>(gf,wd,hf,xch,cnt,(u16*)d_out);
}
```

```cpp
#include <hip/hip_runtime.h>
#include <hip/hip_bf16.h>
#include <type_traits>
typedef unsigned short u16;
typedef __attribute__((ext_vector_type(4))) short s16x4;
typedef __attribute__((ext_vector_type(8))) __bf16 bf16x8;
typedef __attribute__((ext_vector_type(2))) __bf16 bf16x2;
typedef __attribute__((ext_vector_type(2))) float f32x2;
typedef __attribute__((ext_vector_type(2))) unsigned u32x2;
typedef __attribute__((ext_vector_type(4))) unsigned u32x4;
typedef __attribute__((ext_vector_type(16))) float f32x16;
#define LAS __attribute__((address_space(3)))
#define GAS __attribute__((address_space(1)))
#define DI __device__ __forceinline__
#define RLX_AGENT __ATOMIC_RELAXED, __HIP_MEMORY_SCOPE_AGENT

#define STAMP(i)
#define STAMP_DECL
#define STAMP_OUT(k)
constexpr int S=2048, D=576, F=4608;

template<int I,int N,class Fn> DI void static_for(Fn&& f){ if constexpr(I<N){ f(std::integral_constant<int,I>{}); static_for<I+1,N>(f);} }
#define IC(n) std::integral_constant<int,(n)>{}
#define SB() __builtin_amdgcn_sched_barrier(0)
#define MFMA32(a,b,c) __builtin_amdgcn_mfma_f32_32x32x16_bf16((a),(b),(c),0,0,0)
#define BF8(v) __builtin_bit_cast(bf16x8,(v))
DI unsigned pk2(float a,float b){ f32x2 f={a,b}; return __builtin_bit_cast(unsigned,__builtin_convertvector(f,bf16x2)); }
DI float lo16(unsigned u){ return __uint_as_float(u<<16); }
DI float hi16(unsigned u){ return __uint_as_float(u&0xffff0000u); }
DI float opf(float v){ asm("":"+v"(v)); return v; }
DI int lane_id(){ int l; asm volatile("v_mbcnt_lo_u32_b32 %0, -1, 0\n\tv_mbcnt_hi_u32_b32 %0, -1, %0":"=v"(l)); return l; }
template<int N> DI void wait_vm(){ asm volatile("s_waitcnt vmcnt(%0)"::"n"(N):"memory"); }
DI void wait_lgkm0(){ asm volatile("s_waitcnt lgkmcnt(0)":::"memory"); }
DI void bar(){ __builtin_amdgcn_s_barrier(); }
DI s16x4 trd(LAS char* p){ return __builtin_amdgcn_ds_read_tr16_b64_v4i16((LAS s16x4*)p); }
DI bf16x8 cat(s16x4 a,s16x4 b){ return BF8(__builtin_shufflevector(a,b,0,1,2,3,4,5,6,7)); }
template<int TS> DI bf16x8 ldtr(unsigned p){ return cat(trd((LAS char*)(size_t)p),trd((LAS char*)(size_t)p+TS)); }
template<int C> DI unsigned addc(unsigned v){ unsigned r; asm volatile("v_add_u32 %0, %1, %2":"=v"(r):"n"(C),"v"(v)); return r; }
DI void glds16(const char* sb, unsigned voff, unsigned ldsaddr){
  asm volatile("s_mov_b32 m0, %2\n\ts_nop 0\n\tglobal_load_lds_dwordx4 %0, %1"::"v"(voff),"s"(sb),"s"(ldsaddr):"memory"); }
DI void glds16q(const char* sb, unsigned voff, unsigned ldsaddr){
  asm volatile("s_mov_b32 m0, %2\n\ts_mov_b64 exec, 0xffff\n\tglobal_load_lds_dwordx4 %0, %1\n\ts_mov_b64 exec, -1"::"v"(voff),"s"(sb),"s"(ldsaddr):"memory"); }
DI void glds16v(const char* vp, unsigned ldsaddr){
  asm volatile("s_mov_b32 m0, %1\n\ts_nop 0\n\tglobal_load_lds_dwordx4 %0, off"::"v"(vp),"s"(ldsaddr):"memory"); }
DI void gld(u32x4& a,unsigned voff,const char* sb){ asm volatile("global_load_dwordx4 %0, %1, %2":"=&v"(a):"v"(voff),"s"(sb):"memory"); }
DI void gldc(u32x4& a,unsigned voff,const char* sb){ asm volatile("global_load_dwordx4 %0, %1, %2 sc1":"=&v"(a):"v"(voff),"s"(sb):"memory"); }
DI void gld2(f32x2& a,unsigned voff,const char* sb){ asm volatile("global_load_dwordx2 %0, %1, %2":"=&v"(a):"v"(voff),"s"(sb):"memory"); }
DI void gstc(const u32x4& a,unsigned voff,char* sb){ asm volatile("global_store_dwordx4 %1, %0, %2 sc1\n\ts_nop 1"::"v"(a),"v"(voff),"s"(sb):"memory"); }
DI void gstc2(const u32x2& a,unsigned voff,char* sb){ asm volatile("global_store_dwordx2 %1, %0, %2 sc1"::"v"(a),"v"(voff),"s"(sb):"memory"); }
DI void gstc1(unsigned a,unsigned voff,char* sb){ asm volatile("global_store_dword %1, %0, %2 sc1"::"v"(a),"v"(voff),"s"(sb):"memory"); }
template<class T> DI void pin(T& a){ asm volatile("":"+v"(a)); }
template<int N> DI void wait_vm4(u32x4& a,u32x4& b,u32x4& c,u32x4& d){ asm volatile("s_waitcnt vmcnt(%4)":"+v"(a),"+v"(b),"+v"(c),"+v"(d):"n"(N):"memory"); }
template<int N> DI void wait_vm2(u32x4& a,u32x4& b){ asm volatile("s_waitcnt vmcnt(%2)":"+v"(a),"+v"(b):"n"(N):"memory"); }
template<int C> DI float dppf(float v){ return __uint_as_float(__builtin_amdgcn_update_dpp(0u,__float_as_uint(v),C,0xf,0xf,true)); }
DI float sum8(float v){ v+=dppf<0xB1>(v); v+=dppf<0x4E>(v); v+=dppf<0x141>(v); return v; }
DI float xsum32(float v){ const auto r=__builtin_amdgcn_permlane32_swap(__float_as_uint(v),__float_as_uint(v),false,false); return __uint_as_float(r[0])+__uint_as_float(r[1]); }
DI bf16x8 pack8(const f32x16& c,int o){ u32x4 r={pk2(c[o],c[o+1]),pk2(c[o+2],c[o+3]),pk2(c[o+4],c[o+5]),pk2(c[o+6],c[o+7])}; return BF8(r); }

constexpr size_t WS_FLAG=0;
constexpr int    KVP=5632;
constexpr size_t WS_KVP=65536;
constexpr size_t WS_O=WS_KVP+256*KVP;
constexpr size_t WS_H=WS_O+(size_t)S*D*2;
constexpr size_t WS_ST=WS_H+(size_t)S*D*2;
constexpr size_t WS_CS=WS_ST+9*S*16;
constexpr size_t WS_X=WS_CS+32*3*32*16;
constexpr size_t WS_G=WS_X+64*16*9*1024;

DI void wait_progress(const char* ws,int who,int n,unsigned want){
  const int lane=lane_id();
  const GAS unsigned* p=(const GAS unsigned*)(ws+WS_FLAG)+(lane<n?who:0)*32;
  for(unsigned spins=0;spins<(1u<<18);spins++){
    const unsigned v=__hip_atomic_load(p,RLX_AGENT);
    if(__all(lane>=n || v>=want)) break;
    __builtin_amdgcn_s_sleep(1);
  }
}
DI void publish(char* ws,int bx,unsigned v){ __hip_atomic_store((GAS unsigned*)(ws+WS_FLAG)+bx*32,v,RLX_AGENT); }

__global__ __launch_bounds__(512,2) void mega(const char* __restrict__ x,const char* __restrict__ wqkv,const char* __restrict__ wphi,const char* __restrict__ wo,
                                              const char* __restrict__ wg,const char* __restrict__ wu,const char* __restrict__ wd,char* __restrict__ ws,char* __restrict__ outp){
  extern __shared__ __attribute__((aligned(16))) char lds_[];
  LAS char* lds=(LAS char*)lds_; const unsigned lb=(unsigned)(size_t)lds;
  const int w=__builtin_amdgcn_readfirstlane(threadIdx.x>>6);
  const int bx=blockIdx.x, xcd=bx&7, ii=bx>>3;
  STAMP_DECL STAMP(0);
  {
    const int lane=lane_id(), hh=lane>>5, r=lane&31; const unsigned l16=lane*16;
    const int hg=xcd, tk=ii, tg=w&1, hp=(w>>1)&1, role=w>>2;
    constexpr int RB=73728, WST=28672, PHI=RB+3*WST;
    if(w<3){ unsigned o=w*1024+l16; o=o<2592?o:0; glds16(wphi+hg*2592,o,lb+PHI+w*1024); }
    const int n0=(w<4)?w*4:16+(w-4)*3;
    const char* wb=wqkv+hg*144;
    auto body=[&](auto ROLE){ constexpr int ro=decltype(ROLE)::value; constexpr int NL=ro?3:4;
      u32x4 xv[9], wr[2][NL];
      { const char* xs=x+(size_t)(tk*64+w*8)*1152; const unsigned xo=(lane>>3)*1152+(lane&7)*16;
        #pragma unroll
        for(int i=0;i<9;i++) gld(xv[i],xo,xs+i*128); }
      unsigned wo_[NL];
      #pragma unroll
      for(int i=0;i<NL;i++){ const unsigned c=(n0+i)*64+lane, row=c/28, cc=c-row*28, part=cc/9; wo_[i]=row*3456+(cc<27?part*1152+(cc-part*9)*16:0); }
      #pragma unroll
      for(int kb=0;kb<3;kb++){
        #pragma unroll
        for(int i=0;i<NL;i++) glds16(wb+(size_t)kb*64*3456,wo_[i],lb+RB+kb*WST+(n0+i)*1024); }
      #pragma unroll
      for(int kb=3;kb<5;kb++){
        #pragma unroll
        for(int i=0;i<NL;i++) gld(wr[kb&1][i],wo_[i],wb+(size_t)kb*64*3456); }
      {
        wait_vm<5*NL>();
        #pragma unroll
        for(int i=0;i<9;i++) pin(xv[i]);
        float s=0.f;
        #pragma unroll
        for(int i=0;i<9;i++){
          #pragma unroll
          for(int e=0;e<4;e++) s+=lo16(xv[i][e])+hi16(xv[i][e]); }
        s=sum8(s);
        const float m=s*(1.f/D); float q=0.f;
        #pragma unroll
        for(int i=0;i<9;i++){
          #pragma unroll
          for(int e=0;e<4;e++){ const float a=lo16(xv[i][e])-m, b=hi16(xv[i][e])-m; q+=a*a+b*b; } }
        q=sum8(q);
        const float rs=__builtin_amdgcn_rsqf(q*(1.f/D)+1e-5f), sh=-m*rs;
        const int tl=(w&3)*8+(lane>>3), sub=lane&7;
        LAS char* dst=lds+((w>>2)*36+(sub>>1))*1024+((sub&1)*32+(tl^sub))*16;
        #pragma unroll
        for(int i=0;i<9;i++){ u32x4 o;
          #pragma unroll
          for(int e=0;e<4;e++) o[e]=pk2(fmaf(lo16(xv[i][e]),rs,sh),fmaf(hi16(xv[i][e]),rs,sh));
          *(LAS u32x4*)(dst+i*4096)=o; }
      }
      STAMP(1);
      const int q=(lane&15)>>2, p=lane&3, g2=(lane>>4)&1, sg=2*g2+(p>>1);
      const unsigned ab=lb+RB+(8*hh+q)*448+72*hp+32*(p&1);
      const unsigned aA0=ab+ro*144+8*sg;
      const unsigned aA1=ab+(sg==0?ro*144+32:288+8*(sg-1+3*ro>4?4:sg-1+3*ro));
      unsigned aB[4];
      #pragma unroll
      for(int i=0;i<4;i++) aB[i]=lb+tg*36864+(hh*32+(r^(2*i+hh)))*16;
      f32x16 c0={},c1={}; u32x4 pw[6];
      bf16x8 fa0[2],fa1[2],fb[2];
      auto rd=[&](int kb,int s,int b){ const unsigned o=(kb%3)*WST+s*16*448;
        fa0[b]=ldtr<4*448>(aA0+o); fa1[b]=ldtr<4*448>(aA1+o); fb[b]=*(LAS bf16x8*)(size_t)(aB[s]+(kb*4+s)*1024); };
      auto mm=[&](int b){ c0=MFMA32(fa0[b],fb[b],c0); c1=MFMA32(fa1[b],fb[b],c1); SB(); };
      const unsigned wdst=lb+RB+n0*1024+l16;
      static_for<0,9>([&](auto KB){ constexpr int kb=decltype(KB)::value;
        constexpr int n=(kb==0)?4*NL:(kb<=5)?NL:0;
        asm volatile("s_waitcnt vmcnt(%0) lgkmcnt(0)"::"n"(n):"memory"); bar();
        constexpr bool ld=(kb>=1&&kb<=6);
        auto mv=[&](int i){ if(i<NL){ pin(wr[kb&1][i]); *(LAS u32x4*)(size_t)(wdst+((kb+2)%3)*WST+i*1024)=wr[kb&1][i]; if(kb+4<9) gld(wr[kb&1][i],wo_[i],wb+(size_t)(kb+4)*64*3456); } };
        rd(kb,0,0); SB(); if constexpr(kb>0) mm(1); if constexpr(ld) mv(0);
        rd(kb,1,1); SB(); mm(0); if constexpr(ld) mv(1);
        rd(kb,2,0); SB(); mm(1); if constexpr(ld) mv(2);
        rd(kb,3,1); SB(); mm(0); if constexpr(ld) mv(3);
        if constexpr(kb<6){
          const int f=16*(kb/3)+4*(r>>3)+(r&3); const bool ok=(((r>>2)&1)==hh)&&f<18;
          #pragma unroll
          for(int jj=0;jj<4;jj++){ const int d=8*(kb%3)+2*jj-2*hh; const bool v=ok&&d>=0&&d<18;
            const LAS u16* pp=(const LAS u16*)(lds+PHI)+(v?((2*hp+hh)*18+d)*18+f:0);
            const unsigned t=(unsigned)pp[0]|((unsigned)pp[18]<<16); pw[kb][jj]=v?t:0u; }
          SB(); }
      });
      mm(1);
      STAMP(2);
      f32x16 p0={},p1={};
      { const bf16x8 b0=pack8(c0,0), b1=pack8(c0,8), b2=pack8(c1,0);
        p0=MFMA32(BF8(pw[0]),b0,p0); p1=MFMA32(BF8(pw[3]),b0,p1);
        p0=MFMA32(BF8(pw[1]),b1,p0); p1=MFMA32(BF8(pw[4]),b1,p1);
        p0=MFMA32(BF8(pw[2]),b2,p0); p1=MFMA32(BF8(pw[5]),b2,p1); }
      bar();
      const int tl=tg*32+r, sig=(((tl>>1)&1)<<2)|(tl&1)|(((tl>>2)&1)<<1);
      LAS char* trow=lds+((2*hp+hh)*64+tl)*128;
      if constexpr(ro==0){
        *(LAS u32x4*)(trow+((5^sig)*16))=u32x4{pk2(c1[4],c1[5]),pk2(c1[6],c1[7]),pk2(c1[8],c1[9]),pk2(c1[10],c1[11])};
        *(LAS u32x2*)(trow+((6^sig)*16))=u32x2{pk2(c1[12],c1[13]),pk2(c1[14],c1[15])};
      } else {
        *(LAS u32x2*)(trow+((6^sig)*16)+8)=u32x2{pk2(c1[4],c1[5]),pk2(c1[6],c1[7])};
        *(LAS u32x4*)(trow+((7^sig)*16))=u32x4{pk2(c1[8],c1[9]),pk2(c1[10],c1[11]),0x00003f80u,0u};
      }
      unsigned ph[20]; float s1=0.f;
      { float pv[18];
        #pragma unroll
        for(int i=0;i<16;i++) pv[i]=p0[i];
        pv[16]=p1[0]; pv[17]=p1[1];
        float mx=pv[0],mn=pv[0];
        #pragma unroll
        for(int i=1;i<18;i++){ mx=fmaxf(mx,pv[i]); mn=fminf(mn,pv[i]); }
        const float L=1.44269504f, a=-mx*L, b=mn*L; float s2=0.f, e1[18], e2[18];
        #pragma unroll
        for(int i=0;i<18;i++){ e1[i]=__builtin_amdgcn_exp2f(fmaf(pv[i],L,a)); e2[i]=__builtin_amdgcn_exp2f(fmaf(pv[i],-L,b)); s1+=e1[i]; s2+=e2[i]; }
        const float i2=__builtin_amdgcn_rcpf(s2);
        if constexpr(ro==0){ const float a2=s1*i2;
          #pragma unroll
          for(int i=0;i<9;i++){ ph[i]=pk2(e1[2*i],e1[2*i+1]); ph[9+i]=pk2(e2[2*i]*a2,e2[2*i+1]*a2); }
        } else { const float a1=__builtin_amdgcn_rcpf(s1);
          #pragma unroll
          for(int i=0;i<9;i++){ ph[i]=pk2(e1[2*i]*a1,e1[2*i+1]*a1); ph[9+i]=pk2(e2[2*i]*i2,e2[2*i+1]*i2); } }
        ph[18]=0u; ph[19]=0u; }
      if constexpr(ro==1){
        #pragma unroll
        for(int i=0;i<5;i++) *(LAS u32x4*)(trow+((i^sig)*16))=u32x4{ph[4*i],ph[4*i+1],ph[4*i+2],ph[4*i+3]}; }
      { LAS u32x4* z=(LAS u32x4*)(lds+32768)+w*64+lane; z[0]=u32x4{0u,0u,0u,0u}; z[512]=u32x4{0u,0u,0u,0u}; if(w<4) z[1024]=u32x4{0u,0u,0u,0u}; }
      STAMP(3);
      wait_lgkm0(); bar();
      {
        const int hd=w&3; f32x16 kc={};
        #pragma unroll
        for(int s=0;s<4;s++){ s16x4 a[2],b[2];
          #pragma unroll
          for(int t=0;t<2;t++){ const int sx=((q>>1)<<2)|(q&1)|(t<<1);
            LAS char* rp=lds+(hd*64+16*s+8*hh+4*t+q)*128+(p&1)*8;
            a[t]=trd(rp+(((4*ro+sg)^sx)*16)); b[t]=trd(rp+((((5+sg)&7)^sx)*16)); }
          kc=MFMA32(cat(a[0],a[1]),cat(b[0],b[1]),kc); }
        const int rho=r-2*(hd&1), row=(r==20)?18:rho;
        if(r==20 || (rho>=0&&rho<18)){ LAS char* d=lds+57344+(hd*19+row)*72+hh*8;
          if constexpr(ro==0){
            #pragma unroll
            for(int i=0;i<4;i++) *(LAS u32x2*)(d+i*16)=u32x2{pk2(kc[4*i],kc[4*i+1]),pk2(kc[4*i+2],kc[4*i+3])};
          } else if(hh==0) *(LAS u32x2*)(d+64)=u32x2{pk2(kc[0],kc[1]),pk2(kc[2],kc[3])}; }
      }
      wait_lgkm0(); bar();
      if(w<6){ const u32x4 v=*(LAS u32x4*)(lds+57344+w*1024+l16); if(w*64+lane<352) gstc(v,w*1024+l16,ws+WS_KVP+(size_t)(hg*32+tk)*KVP); wait_vm<0>(); }
      STAMP(4);
      bar();
      auto slab=[&](){ if(ii<18){
        const int u=xcd*18+ii, np=u>>4;
        const int rl=8*w+(lane>>3), hk=(rl>>3)&1, j=rl&7; const unsigned co=((lane&7)^(((lane>>4)&1)<<2))*16;
        #pragma unroll
        for(int kb=0;kb<9;kb++){ const int s=kb*4+(rl>>4);
          const int row=(kb<8)?36*(s>>1)+18*hk+8*(s&1)+j:36*(4*(s-32)+(j>>1))+18*hk+16+(j&1);
          glds16(wo+np*128,row*1152+co,lb+RB+kb*8192+w*1024); } } };
      if(w==7){ if(lane==0) publish(ws,bx,1); wait_progress(ws,lane*8+hg,32,1); }
      slab();
      bar();
      STAMP(5);
      if(w<6){ const int c=w*64+lane; const unsigned vo=(c<342?c:0)*16;
        float a[8]={0.f,0.f,0.f,0.f,0.f,0.f,0.f,0.f};
        u32x4 pv[32];
        #pragma unroll
        for(int i=0;i<32;i++) gldc(pv[i],vo,ws+WS_KVP+(size_t)(hg*32+((i+tk)&31))*KVP);
        static_for<0,8>([&](auto G){ constexpr int g=decltype(G)::value;
          wait_vm4<28-4*g>(pv[4*g],pv[4*g+1],pv[4*g+2],pv[4*g+3]);
          #pragma unroll
          for(int i=4*g;i<4*g+4;i++){
            #pragma unroll
            for(int e=0;e<4;e++){ a[2*e]+=lo16(pv[i][e]); a[2*e+1]+=hi16(pv[i][e]); } } });
        if(c<342){
          #pragma unroll
          for(int hf=0;hf<2;hf++){ const int g=2*c+hf, hd=g/171, rem=g-hd*171, rho=rem/9, f4=rem-rho*9;
            const int ph_=rho&15, m=8*(ph_>>2)+4*(hd&1)+(ph_&3);
            *(LAS u32x2*)(lds+32768+((((hd>>1)*2+(rho>>4))*5+(f4>>1))*64+(hd&1)*32+m)*16+(f4&1)*8)=u32x2{pk2(a[4*hf],a[4*hf+1]),pk2(a[4*hf+2],a[4*hf+3])}; } } }
      wait_lgkm0(); bar();
      STAMP(6);
      if constexpr(ro==0){
        f32x16 o0={},o1={};
        #pragma unroll
        for(int kp=0;kp<5;kp++){ const bf16x8 b=BF8((u32x4{ph[4*kp],ph[4*kp+1],ph[4*kp+2],ph[4*kp+3]}));
          o0=MFMA32(*(LAS bf16x8*)(lds+32768+(hp*10+kp)*1024+l16),b,o0); o1=MFMA32(*(LAS bf16x8*)(lds+32768+(hp*10+5+kp)*1024+l16),b,o1); }
        const float id=__builtin_amdgcn_rcpf(fmaf(s1,1e-6f,o1[2]));
        char* dst=ws+WS_O+(size_t)(tk*2+tg)*36*1024; const int pr=2*hg+hp;
        gstc(u32x4{pk2(o0[0]*id,o0[1]*id),pk2(o0[2]*id,o0[3]*id),pk2(o0[4]*id,o0[5]*id),pk2(o0[6]*id,o0[7]*id)},l16,dst+(2*pr)*1024);
        gstc(u32x4{pk2(o0[8]*id,o0[9]*id),pk2(o0[10]*id,o0[11]*id),pk2(o0[12]*id,o0[13]*id),pk2(o0[14]*id,o0[15]*id)},l16,dst+(2*pr+1)*1024);
        gstc1(pk2(o1[0]*id,o1[1]*id),l16,dst+(32+(pr>>2))*1024+(pr&3)*4);
        wait_vm<0>(); }
    };
    if(role==0) body(IC(0)); else body(IC(1));
    bar();
    if(w==0 && lane==0) publish(ws,bx,2);
  }
  STAMP(7);
  {
    const int lane=lane_id(), j=8*(xcd>>1)+(ii>>2);
    const unsigned voffS=(unsigned)((8*w+(lane>>3))*(F*2)+(((lane&7)^(((lane>>4)&1)<<2))*16));
    #pragma unroll
    for(int kb=0;kb<3;kb++){ glds16(wg+j*256+(size_t)kb*64*F*2,voffS,lb+kb*16384+w*1024); glds16(wu+j*256+(size_t)kb*64*F*2,voffS,lb+kb*16384+8192+w*1024); } }
  if(ii<18){
    const int lane=lane_id(), hh=lane>>5, r=lane&31; const unsigned l16=lane*16;
    const int u=xcd*18+ii, np=u>>4, tk=u&15, tg=w&3, kh=w>>2;
    f32x2 xr[4];
    { const char* xs=x+((size_t)(tk*128+tg*32)*576+np*64+kh*32)*2;
      #pragma unroll
      for(int i=0;i<4;i++) gld2(xr[i],r*1152+hh*8,xs+i*16); }
    if(w==0) wait_progress(ws,(2*tk+(lane>>3&1))*8+(lane&7),16,2);
    wait_vm<0>(); bar();
    STAMP(8);
    u32x4 ov[18];
    { const char* os=ws+WS_O+((size_t)(tk*4+tg)*36+kh*18)*1024;
      #pragma unroll
      for(int i=0;i<18;i++) gldc(ov[i],l16,os+i*1024); }
    const int q=(lane&15)>>2, p=lane&3, g2=(lane>>4)&1;
    const unsigned a0_=(8*hh+q)*128+(((2*g2+(p>>1))^((q>>1)<<2))*16)+(p&1)*8, a0=lb+73728+kh*18*2048+a0_, a1=lb+73728+kh*18*2048+(a0_^64);
    f32x16 c0={},c1={};
    static_for<0,9>([&](auto G){ constexpr int g=decltype(G)::value;
      const bf16x8 f0=ldtr<512>(a0+g*4096), f1=ldtr<512>(a1+g*4096), f2=ldtr<512>(a0+g*4096+2048), f3=ldtr<512>(a1+g*4096+2048);
      wait_vm2<16-2*g>(ov[2*g],ov[2*g+1]);
      c0=MFMA32(f0,BF8(ov[2*g]),c0); c1=MFMA32(f1,BF8(ov[2*g]),c1); c0=MFMA32(f2,BF8(ov[2*g+1]),c0); c1=MFMA32(f3,BF8(ov[2*g+1]),c1); });
    {
      LAS char* sx=lds+(w<6?49152+w*4096:147456+(w-6)*4096)+l16; LAS char* rx=lds+((w^4)<6?49152+(w^4)*4096:147456+((w^4)-6)*4096)+l16;
      #pragma unroll
      for(int i=0;i<4;i++) *(LAS u32x4*)(sx+i*1024)=kh?u32x4{__float_as_uint(c0[4*i]),__float_as_uint(c0[4*i+1]),__float_as_uint(c0[4*i+2]),__float_as_uint(c0[4*i+3])}
                                                       :u32x4{__float_as_uint(c1[4*i]),__float_as_uint(c1[4*i+1]),__float_as_uint(c1[4*i+2]),__float_as_uint(c1[4*i+3])};
      wait_lgkm0(); bar();
      #pragma unroll
      for(int i=0;i<4;i++){ const u32x4 v=*(LAS u32x4*)(rx+i*1024);
        #pragma unroll
        for(int e=0;e<4;e++) c0[4*i+e]=(kh?c1[4*i+e]:c0[4*i+e])+__uint_as_float(v[e]); } }
    pin(xr[0]); pin(xr[1]); pin(xr[2]); pin(xr[3]);
    unsigned o[8]; float v[16], sm=0.f;
    #pragma unroll
    for(int i=0;i<4;i++){ const unsigned x0=__float_as_uint(xr[i][0]), x1=__float_as_uint(xr[i][1]);
      o[2*i]=pk2(c0[4*i]+lo16(x0),c0[4*i+1]+hi16(x0)); o[2*i+1]=pk2(c0[4*i+2]+lo16(x1),c0[4*i+3]+hi16(x1)); }
    char* dst=ws+WS_H+((size_t)(tk*4+tg)*36+np*4+kh*2)*1024;
    gstc(u32x4{o[0],o[1],o[2],o[3]},l16,dst); gstc(u32x4{o[4],o[5],o[6],o[7]},l16,dst+1024);
    #pragma unroll
    for(int i=0;i<8;i++){ v[2*i]=lo16(o[i]); v[2*i+1]=hi16(o[i]); sm+=v[2*i]+v[2*i+1]; }
    sm=xsum32(sm)*(1.f/32);
    float qq=0.f;
    #pragma unroll
    for(int i=0;i<16;i++){ const float d=v[i]-sm; qq+=d*d; }
    qq=xsum32(qq);
    if(hh==0) gstc2(u32x2{__float_as_uint(sm),__float_as_uint(qq)},r*16+kh*8,ws+WS_ST+((size_t)np*S+tk*128+tg*32)*16);
    wait_vm<0>(); bar();
    if(w==0 && lane==0) publish(ws,bx,3);
  } else if(ii<27){
    const int lane=lane_id();
    const int u=(ii-18)*8+xcd, m=u/36, cb=u-m*36;
    const char* src=(m?wu:wg)+cb*256+(size_t)w*72*(F*2);
    float a[8]={0.f,0.f,0.f,0.f,0.f,0.f,0.f,0.f};
    u32x4 v[18];
    #pragma unroll
    for(int i=0;i<18;i++) gld(v[i],(lane>>4)*(F*2)+(lane&15)*16,src+(size_t)i*4*(F*2));
    wait_vm<0>();
    #pragma unroll
    for(int i=0;i<18;i++){ pin(v[i]);
      #pragma unroll
      for(int e=0;e<4;e++){ a[2*e]+=lo16(v[i][e]); a[2*e+1]+=hi16(v[i][e]); } }
    #pragma unroll
    for(int e=0;e<8;e++){ a[e]+=__shfl_xor(a[e],16); a[e]+=__shfl_xor(a[e],32); }
    LAS float* red=(LAS float*)(lds+65536);
    if(lane<16){
      #pragma unroll
      for(int e=0;e<8;e++) red[w*128+lane*8+e]=a[e]; }
    wait_lgkm0(); bar();
    if(w<2){ const int t_=w*64+lane; float s=0.f;
      #pragma unroll
      for(int i=0;i<8;i++) s+=red[i*128+t_];
      const unsigned hi=pk2(s,s)&0xffffu, lo=pk2(s-lo16(hi),0.f)&0xffffu;
      const int f=cb*128+t_; int j,t,l;
      if(f<4096){ j=f>>7; t=((f>>5)&3)*2+m; l=f&31; } else { j=(f-4096)>>4; t=8; l=(f&15)+16*m; }
      gstc1(hi|(lo<<16),(((j*3+(t>>2))*32+l)*4+(t&3))*4,ws+WS_CS); wait_vm<0>(); }
    bar();
    if(w==0 && lane==0) publish(ws,bx,3);
  } else {
    const int lane=lane_id(), sk=xcd>>1;
    u32x4 v[16];
    #pragma unroll
    for(int i=0;i<16;i++){ const int o=((((xcd&1)*5+ii-27)*8+w)*16+i)*1024;
      gld(v[i],lane*16,wd+(o<1024*1152?(size_t)sk*1024*1152+o:(size_t)(4096+128*sk)*1152+(o-1024*1152))); }
    wait_vm<0>();
    #pragma unroll
    for(int i=0;i<16;i++) pin(v[i]);
  }
  STAMP(9);
  {
    const int lane=lane_id();
    const int tt=4*(xcd&1)+(ii&3), j=8*(xcd>>1)+(ii>>2), T=tt*8+w;
    const unsigned voffS=(unsigned)((8*w+(lane>>3))*(F*2)+(((lane&7)^(((lane>>4)&1)<<2))*16));
    const char* gA=wg+j*256; const char* uA=wu+j*256;
    const unsigned xoff=lane*16; const char* xb=ws+WS_H+(size_t)T*36*1024;
    u32x4 x_[36], sp[5], cv[3];
    auto ldw=[&](int m,int kb,int ch,int ps){ glds16((m?uA:gA)+ch*128+(size_t)kb*64*F*2, voffS, lb+ps*16384+m*8192+w*1024); };
    if(w==0){
      int who; if(lane<18){ const int u=(lane>>1)*16+2*tt+(lane&1); who=(u%18)*8+u/18; } else { const int u=(lane&1)*36+((lane&2)?32+(j>>3):j); who=(18+(u>>3))*8+(u&7); }
      wait_progress(ws,who,22,3); }
    bar();
    STAMP(10);
    #pragma unroll
    for(int i=0;i<8;i++) gldc(x_[i],xoff,xb+i*1024);
    { const unsigned l16=(lane&31)*16, so=l16+(lane>>5)*(4*S*16); const char* sb=ws+WS_ST+(size_t)T*32*16; const char* cs=ws+WS_CS+j*1536;
      gldc(sp[0],so,sb); gldc(sp[1],so,sb+S*16); gldc(sp[2],so,sb+2*S*16); gldc(sp[3],so,sb+3*S*16); gldc(sp[4],l16,sb+8*S*16);
      gldc(cv[0],l16,cs); gldc(cv[1],l16,cs+512); gldc(cv[2],l16,cs+1024); }
    #pragma unroll
    for(int i=8;i<12;i++) gldc(x_[i],xoff,xb+i*1024);
    if(w>=4) __builtin_amdgcn_s_setprio(1);
    const int q=(lane&15)>>2, p=lane&3, g2=(lane>>4)&1, hh=lane>>5;
    const unsigned a0_=(4*hh+q)*128+(((2*g2+(p>>1))^((q>>1)<<2))*16)+(p&1)*8, a0=lb+a0_, a1=lb+(a0_^64);
    const unsigned ac=lb+(4*hh+q)*64+(2*g2+(p>>1))*16+(p&1)*8;
    auto ldc=[&](int pi,int sl){ const unsigned l4=lane_id(); const int row0=pi*128+16*w-((pi==4&&w>=4)?64:0);
      const char* m=((l4&2)?wu:wg)+(size_t)(4096+16*j)*2+(size_t)row0*(F*2);
      glds16v(m+((l4>>2)*(F*2)+(l4&1)*16), lb+sl*8192+w*1024); };
    f32x16 acc[4]={};
    char* outb=ws+WS_G+(size_t)T*288*1024;
    constexpr int PD=4, NU=324;
    bf16x8 A[PD]; u32x4 eo, bm; float k1, iv, sm;
    auto unit=[](int u,int& kb,int& s,int& t){
      if(u<96){ kb=u>>4; s=(u>>2)&3; t=u&3; }
      else if(u<120){ int v=u-96; kb=6+(v>>3); s=(v>>1)&3; t=v&1; }
      else if(u<144){ int v=u-120; kb=6+(v>>3); s=(v>>1)&3; t=2+(v&1); }
      else if(u<216){ int v=u-144; kb=v>>3; s=(v>>1)&3; t=v&1; }
      else if(u<288){ int v=u-216; kb=v>>3; s=(v>>1)&3; t=2+(v&1); }
      else { int v=u-288; kb=v>>2; s=v&3; t=0; } };
    unsigned pb0=0,pb1=0;
    auto rd=[&](auto U){ constexpr int u=decltype(U)::value; int kb=0,s=0,t=0; unit(u,kb,s,t);
      if constexpr(u<288){ constexpr int kbc=(u<96)?(u>>4):(u<120)?6+((u-96)>>3):(u<144)?6+((u-120)>>3):(u<216)?((u-144)>>3):((u-216)>>3); constexpr int ps=(u<144)?kbc:(kbc+9)%10;
        if constexpr(u<96){ if constexpr((u&15)==0) pb0=addc<ps*16384>(a0); if constexpr((u&15)==2) pb1=addc<ps*16384>(a1); }
        else if constexpr((u&7)==0){ if constexpr(u<120||(u>=144&&u<216)) pb0=addc<ps*16384>(a0); else pb1=addc<ps*16384>(a1); }
        A[u%PD]=ldtr<1024>(((t&2)?pb1:pb0)+(t&1)*8192+s*2048); }
      else { constexpr int v=u-288, pi=v/8, sl=(pi<4)?16+pi:0;
        if constexpr((v&7)==0) pb0=addc<sl*8192>(ac);
        A[u%PD]=ldtr<512>(pb0+(v%8)*1024); } };
    float ea[2], eb[2], ep;
    auto job=[&](auto I,auto NE,const f32x16& g,const f32x16& u,int uo,int fs){ constexpr int i=decltype(I)::value, ne=decltype(NE)::value;
      constexpr int k=i/3, st=i%3, e=k-st;
      if constexpr(e>=0 && e<ne){
        if constexpr(st==0) ea[e&1]=__builtin_amdgcn_exp2f(g[e]*k1);
        if constexpr(st==1) eb[e&1]=__builtin_amdgcn_rcpf(fmaf(ea[e&1],iv,iv));
        if constexpr(st==2){ float v=opf(g[e]*u[uo+e]*eb[e&1]);
          if constexpr((e&1)==0) ep=v; else eo[(e>>1)&3]=pk2(ep,v);
          if constexpr((e&7)==7) gstc(eo,xoff,outb+(fs+(e>>3))*1024); } } };
    auto spread=[&](auto U,auto U0,auto L,const f32x16& g,const f32x16& uu,int fs){ constexpr int u=decltype(U)::value,u0=decltype(U0)::value,l=decltype(L)::value;
      if constexpr(u>=u0 && u<u0+l){ constexpr int b=(u-u0)*54/l, e=(u-u0+1)*54/l; static_for<b,e>([&](auto I){ job(I,IC(16),g,uu,0,fs); }); } };
    auto corr=[&](unsigned d,const f32x16& c){ u32x4 a={d,d&0xffffu,0u,0u}; return MFMA32(BF8(a),BF8(bm),c); };
    static_for<-PD,NU>([&](auto U){ constexpr int u=decltype(U)::value; constexpr int r=u+PD;
      if constexpr(r<=96 && (r&15)==0){ constexpr int kb=r>>4;
        constexpr int n=(kb==0)?16:(kb==1)?18:12;
        wait_vm4<n>(x_[4*kb],x_[4*kb+1],x_[4*kb+2],x_[4*kb+3]);
        bar(); }
      if constexpr(r==104){ wait_vm4<9>(x_[28],x_[29],x_[30],x_[31]); bar(); }
      if constexpr(r==112){ wait_vm4<6>(x_[32],x_[33],x_[34],x_[35]); bar(); }
      if constexpr(r==144){ wait_vm<3>(); bar(); STAMP(11); }
      if constexpr(r==144+40){ wait_vm<2>(); bar(); }
      if constexpr(r==288){ wait_vm<0>(); bar(); }
      if constexpr(u>=0){ int kb=0,s=0,t=0; unit(u,kb,s,t);
        if constexpr(u==144||u==145) acc[u-144]=corr(cv[1][u-144],f32x16{});
        if constexpr(u==216||u==217) acc[u-214]=corr(cv[1][2+u-216],f32x16{});
        if constexpr(u==288) acc[0]=corr(cv[2][0],f32x16{});
        if constexpr(u>=56&&u<60) acc[u-56]=corr(cv[0][u-56],acc[u-56]);
        acc[t]=MFMA32(A[u%PD],BF8(x_[u<288?4*kb+s:u-288]),acc[t]);
        SB(); }
      if constexpr(r<NU) rd(IC(r));
      if constexpr(r<96){ constexpr int kb=r>>4, o=r&15; constexpr int slot=(o==1)?0:(o==3)?1:(o==6)?2:(o==8)?3:(o==11)?4:(o==13)?5:-1;
        if constexpr(slot>=0){ if constexpr(slot<2) ldw(slot,kb+3,0,kb+3); else gldc(x_[4*(kb+3)+slot-2],xoff,xb+(4*(kb+3)+slot-2)*1024); } }
      else if constexpr(r<128){ constexpr int o=(r-96)&7; constexpr int slot=(o==1)?0:(o==3)?1:(o==6)?2:-1;
        if constexpr(slot>=0){ constexpr int n=((r-96)>>3)*3+slot, i=n>>1; ldw(n&1,i,1,i==0?9:i-1); } }
      if constexpr(r==130) ldw(0,6,1,5);
      if constexpr(r==133) ldw(1,6,1,5);
      if constexpr(r>=36 && r<48){
        if constexpr(r==36){ pin(sp[0]); pin(sp[1]); pin(sp[2]); pin(sp[3]); pin(sp[4]); pin(cv[0]); pin(cv[1]); pin(cv[2]); }
        if constexpr(r==37){ float a=__uint_as_float(hh?sp[4][2]:sp[4][0]);
          #pragma unroll
          for(int i=0;i<4;i++) a+=__uint_as_float(sp[i][0])+__uint_as_float(sp[i][2]);
          sm=xsum32(a)*(1.f/18); }
        if constexpr(r==41){ float d=__uint_as_float(hh?sp[4][2]:sp[4][0])-sm; float a=__uint_as_float(hh?sp[4][3]:sp[4][1]), b=d*d;
          #pragma unroll
          for(int i=0;i<8;i++){ d=__uint_as_float(sp[i>>1][(i&1)*2])-sm; a+=__uint_as_float(sp[i>>1][(i&1)*2+1]); b=fmaf(d,d,b); }
          a=fmaf(b,32.f,a);
          iv=xsum32(a)*(1.f/D)+1e-5f; }
        if constexpr(r==46){ k1=__builtin_amdgcn_rsqf(iv)*-1.44269504f;
          const unsigned mh=pk2(-sm,-sm); const float ml=-sm-lo16(mh);
          bm=u32x4{hh?0u:mh, hh?0u:(pk2(ml,ml)&0xffffu), 0u, 0u}; } }
      if constexpr(r==144+1) ldw(0,7,1,6);
      if constexpr(r==144+4) ldw(1,7,1,6);
      if constexpr(r==144+7) ldw(0,8,1,7);
      if constexpr(r==144+10) ldw(1,8,1,7);
      if constexpr(r==144+42) ldc(0,16);
      if constexpr(r==144+46) ldc(1,17);
      if constexpr(u==216+15){ SB(); bar(); }
      if constexpr(u==216+16) ldc(2,18);
      if constexpr(u==216+19) ldc(3,19);
      if constexpr(u==216+22) ldc(4,0);
      spread(U,IC(121),IC(23),acc[0],acc[1],8*j);
      spread(U,IC(146),IC(34),acc[2],acc[3],8*j+2);
      spread(U,IC(218),IC(66),acc[0],acc[1],8*j+4);
      spread(U,IC(290),IC(34),acc[2],acc[3],8*j+6);
      SB();
    });
    static_for<0,30>([&](auto I){ job(I,IC(8),acc[0],acc[0],8,256+j); });
    __builtin_amdgcn_s_setprio(0);
    wait_vm<0>(); bar();
    if(w==0 && lane==0) publish(ws,bx,5);
  }
  STAMP(12);
  {
    const int lane=lane_id(); const unsigned l16=lane*16;
    const int sk=xcd>>1, tt=8*(xcd&1)+(ii&7), ng=ii>>3, tg=w&3, kh=w>>2;
    constexpr int ST=18432, LA=4;
    const unsigned voffW=(unsigned)((8*w+(lane>>3))*(D*2)+(((lane&7)^(((lane>>4)&1)<<2))*16));
    const unsigned voffQ=(unsigned)((8*w+((lane&15)>>1))*(D*2)+(lane&1)*16+1024+ng*32);
    const char* hb=ws+WS_G+((size_t)(tt*4+tg)*288+2*kh)*1024;
    u32x4 hv[LA][2];
    auto f0of=[&](int kb){ return (kb<16)?1024*sk+64*kb:4096+128*sk+64*(kb-16); };
    auto ldW=[&](int kb,int pc){ const unsigned L=lb+(kb&7)*ST; const char* wsrc=wd+(size_t)f0of(kb)*D*2;
      if(pc==0) glds16(wsrc+ng*256,voffW,L+w*1024);
      if(pc==1) glds16(wsrc+ng*256+128,voffW,L+8192+w*1024);
      if(pc==2) glds16q(wsrc,voffQ,L+16384+w*256); };
    #pragma unroll
    for(int kb=0;kb<LA;kb++){ ldW(kb,0); ldW(kb,1); ldW(kb,2); }
    if(w==0) wait_progress(ws,((lane&7)*4+((tt>>1)&3))*8+xcd,8,5);
    bar();
    STAMP(13);
    #pragma unroll
    for(int kb=0;kb<LA;kb++){ gldc(hv[kb][0],l16,hb+(size_t)(f0of(kb)>>4)*1024); gldc(hv[kb][1],l16,hb+(size_t)(f0of(kb)>>4)*1024+1024); }
    if(w>=4) __builtin_amdgcn_s_setprio(1);
    const int q=(lane&15)>>2, p=lane&3, g2=(lane>>4)&1, hh=lane>>5;
    const unsigned a0_=(4*hh+q)*128+(((2*g2+(p>>1))^((q>>1)<<2))*16)+(p&1)*8, a0=lb+kh*4096+a0_, a1=lb+kh*4096+(a0_^64);
    const unsigned aq=lb+16384+kh*1024+(4*hh+q)*32+(p>>1)*16+(p&1)*8;
    f32x16 c[5]={};
    constexpr int PD=4, NU=180;
    bf16x8 A[PD]; u32x4 hc[2]; unsigned pb0=0,pb1=0,pbq=0;
    static_for<-PD,NU>([&](auto U){ constexpr int u=decltype(U)::value, r=u+PD;
      if constexpr(r<NU && r%10==0){ constexpr int kb=r/10;
        constexpr int n=(kb<LA)?(LA-1-kb)*2+kb*5:((17-kb<LA-1)?(17-kb):(LA-1))*5;
        wait_vm2<n>(hv[kb%LA][0],hv[kb%LA][1]); bar();
        pb0=addc<(kb&7)*ST>(a0); pb1=addc<(kb&7)*ST>(a1); pbq=addc<(kb&7)*ST>(aq); }
      if constexpr(u>=0){ constexpr int kb=u/10, s=(u%10)/5, t=u%5;
        if constexpr(u%10==0){ hc[0]=hv[kb%LA][0]; hc[1]=hv[kb%LA][1]; }
        c[t]=MFMA32(A[u%PD],BF8(hc[s]),c[t]); SB(); }
      if constexpr(r<NU){ constexpr int s=(r%10)/5, t=r%5;
        if constexpr(t<4) A[r%PD]=ldtr<1024>(((t&1)?pb1:pb0)+(t>>1)*8192+s*2048); else A[r%PD]=ldtr<256>(pbq+s*512); }
      if constexpr(u>=0 && u/10+LA<18){ constexpr int kb=u/10+LA, o=u%10;
        if constexpr(o==0) ldW(kb,0); if constexpr(o==2) ldW(kb,1); if constexpr(o==4) ldW(kb,2);
        if constexpr(o==1) gldc(hv[kb%LA][0],l16,hb+(size_t)(f0of(kb)>>4)*1024);
        if constexpr(o==3) gldc(hv[kb%LA][1],l16,hb+(size_t)(f0of(kb)>>4)*1024+1024); }
      SB();
    });
    __builtin_amdgcn_s_setprio(0);
    STAMP(14);
    bar();
    { LAS char* sx=lds+w*10240+l16; LAS char* rx=lds+(w^4)*10240+l16;
      auto put=[&](const f32x16& a,int g,int o){ *(LAS u32x4*)(sx+o*1024)=u32x4{__float_as_uint(a[4*g]),__float_as_uint(a[4*g+1]),__float_as_uint(a[4*g+2]),__float_as_uint(a[4*g+3])}; };
      auto get=[&](f32x16& a,int g,int o){ const u32x4 v=*(LAS u32x4*)(rx+o*1024);
        #pragma unroll
        for(int e=0;e<4;e++) a[4*g+e]+=__uint_as_float(v[e]); };
      if(kh){
        #pragma unroll
        for(int g=0;g<4;g++){ put(c[0],g,g); put(c[1],g,4+g); }
        put(c[4],0,8); put(c[4],1,9);
        c[0]=c[2]; c[1]=c[3];
      } else {
        #pragma unroll
        for(int g=0;g<4;g++){ put(c[2],g,g); put(c[3],g,4+g); } }
      wait_lgkm0(); bar();
      #pragma unroll
      for(int g=0;g<4;g++){ get(c[0],g,g); get(c[1],g,4+g); }
      if(!kh){ get(c[4],0,8); get(c[4],1,9); } }
    const int tile=tt*4+ng;
    char* xb=ws+WS_X+(size_t)(tile*4+tg)*4*9216;
    u32x4 rr[5];
    if(tg!=sk){ char* d=xb+sk*9216+kh*4096;
      gstc(__builtin_bit_cast(u32x4,pack8(c[0],0)),l16,d); gstc(__builtin_bit_cast(u32x4,pack8(c[0],8)),l16,d+1024);
      gstc(__builtin_bit_cast(u32x4,pack8(c[1],0)),l16,d+2048); gstc(__builtin_bit_cast(u32x4,pack8(c[1],8)),l16,d+3072);
      if(!kh) gstc(__builtin_bit_cast(u32x4,pack8(c[4],0)),l16,d+8192);
    } else { const char* hr=ws+WS_H+(size_t)(tt*4+tg)*36*1024;
      #pragma unroll
      for(int i=0;i<4;i++) gldc(rr[i],l16,hr+(ng*8+kh*4+i)*1024);
      gldc(rr[4],l16,hr+(32+ng)*1024); }
    wait_vm<0>(); bar();
    if(w==0 && lane==0) publish(ws,bx,6);
    if(tg==sk){
      wait_progress(ws,ii*8+(lane&3)*2+(xcd&1),4,6);
      STAMP(15);
      u32x4 pv[3][5];
      #pragma unroll
      for(int s=0;s<3;s++){ const char* d=xb+(s+(s>=sk?1:0))*9216;
        #pragma unroll
        for(int i=0;i<4;i++) gldc(pv[s][i],l16,d+kh*4096+i*1024);
        gldc(pv[s][4],l16,d+8192); }
      wait_vm<0>();
      #pragma unroll
      for(int i=0;i<5;i++){ pin(rr[i]); pin(pv[0][i]); pin(pv[1][i]); pin(pv[2][i]); }
      char* orow=outp+((size_t)((tt*4+tg)*32+(lane&31))*D+4*hh)*2;
      auto fin=[&](const f32x16& a,int half,int fi,int n0){
        float v[8];
        #pragma unroll
        for(int e=0;e<4;e++){
          v[2*e]=a[8*half+2*e]+lo16(rr[fi][e])+lo16(pv[0][fi][e])+lo16(pv[1][fi][e])+lo16(pv[2][fi][e]);
          v[2*e+1]=a[8*half+2*e+1]+hi16(rr[fi][e])+hi16(pv[0][fi][e])+hi16(pv[1][fi][e])+hi16(pv[2][fi][e]); }
        *(GAS u32x2*)(orow+n0*2)=u32x2{pk2(v[0],v[1]),pk2(v[2],v[3])}; *(GAS u32x2*)(orow+n0*2+16)=u32x2{pk2(v[4],v[5]),pk2(v[6],v[7])}; };
      const int nb=ng*128+kh*64;
      fin(c[0],0,0,nb); fin(c[0],1,1,nb+16); fin(c[1],0,2,nb+32); fin(c[1],1,3,nb+48);
      if(!kh) fin(c[4],0,4,512+ng*16);
    }
  }
  STAMP_OUT(0);
}

extern "C" void kernel_launch(void* const* d_in, const int* in_sizes, int n_in, void* d_out, int out_size, void* d_ws, size_t ws_size, hipStream_t stream){
  (void)hipFuncSetAttribute((const void*)mega,hipFuncAttributeMaxDynamicSharedMemorySize,163840);
  (void)hipMemsetAsync(d_ws,0,32768,stream);
  mega<<<256,512,163840,stream>>>((const char*)d_in[0],(const char*)d_in[1],(const char*)d_in[2],(const char*)d_in[3],(const char*)d_in[5],(const char*)d_in[6],(const char*)d_in[7],(char*)d_ws,(char*)d_out);
}
```

```cpp
#include <hip/hip_runtime.h>
#include <hip/hip_bf16.h>
#include <type_traits>
typedef unsigned short u16;
typedef __attribute__((ext_vector_type(4))) short s16x4;
typedef __attribute__((ext_vector_type(8))) __bf16 bf16x8;
typedef __attribute__((ext_vector_type(2))) __bf16 bf16x2;
typedef __attribute__((ext_vector_type(2))) float f32x2;
typedef __attribute__((ext_vector_type(2))) unsigned u32x2;
typedef __attribute__((ext_vector_type(4))) unsigned u32x4;
typedef __attribute__((ext_vector_type(4))) float f32x4;
typedef __attribute__((ext_vector_type(16))) float f32x16;
#define LAS __attribute__((address_space(3)))
#define GAS __attribute__((address_space(1)))
#define DI __device__ __forceinline__
#define RLX_AGENT __ATOMIC_RELAXED, __HIP_MEMORY_SCOPE_AGENT

#define STAMP(i)
#define PST(d,i)
#define STAMP_DECL
#define STAMP_OUT(k)
constexpr unsigned CODE_PF=65536;
#define KPRIO 0
constexpr int S=2048, D=576, F=4608;

template<int I,int N,class Fn> DI void static_for(Fn&& f){ if constexpr(I<N){ f(std::integral_constant<int,I>{}); static_for<I+1,N>(f);} }
#define IC(n) std::integral_constant<int,(n)>{}
#define SB() __builtin_amdgcn_sched_barrier(0)
#define MFMA32(a,b,c) __builtin_amdgcn_mfma_f32_32x32x16_bf16((a),(b),(c),0,0,0)
#define MFMA16(a,b,c) __builtin_amdgcn_mfma_f32_16x16x32_bf16((a),(b),(c),0,0,0)
#define BF8(v) __builtin_bit_cast(bf16x8,(v))
DI unsigned pk2(float a,float b){ f32x2 f={a,b}; return __builtin_bit_cast(unsigned,__builtin_convertvector(f,bf16x2)); }
DI float lo16(unsigned u){ return __uint_as_float(u<<16); }
DI float hi16(unsigned u){ return __uint_as_float(u&0xffff0000u); }
DI float opf(float v){ asm("":"+v"(v)); return v; }
DI int lane_id(){ int l; asm volatile("v_mbcnt_lo_u32_b32 %0, -1, 0\n\tv_mbcnt_hi_u32_b32 %0, -1, %0":"=v"(l)); return l; }
template<int N> DI void wait_vm(){ asm volatile("s_waitcnt vmcnt(%0)"::"n"(N):"memory"); }
DI void wait_lgkm0(){ asm volatile("s_waitcnt lgkmcnt(0)":::"memory"); }
DI void bar(){ __builtin_amdgcn_s_barrier(); }
DI s16x4 trd(LAS char* p){ return __builtin_amdgcn_ds_read_tr16_b64_v4i16((LAS s16x4*)p); }
DI bf16x8 cat(s16x4 a,s16x4 b){ return BF8(__builtin_shufflevector(a,b,0,1,2,3,4,5,6,7)); }
template<int TS> DI bf16x8 ldtr(unsigned p){ return cat(trd((LAS char*)(size_t)p),trd((LAS char*)(size_t)p+TS)); }
template<int C> DI unsigned addc(unsigned v){ unsigned r; asm volatile("v_add_u32 %0, %1, %2":"=v"(r):"n"(C),"v"(v)); return r; }
DI void glds16(const char* sb, unsigned voff, unsigned ldsaddr){
  asm volatile("s_mov_b32 m0, %2\n\ts_nop 0\n\tglobal_load_lds_dwordx4 %0, %1"::"v"(voff),"s"(sb),"s"(ldsaddr):"memory"); }
DI void glds4(const char* sb, unsigned voff, unsigned ldsaddr){
  asm volatile("s_mov_b32 m0, %2\n\ts_nop 0\n\tglobal_load_lds_dword %0, %1"::"v"(voff),"s"(sb),"s"(ldsaddr):"memory"); }
DI void glds16q(const char* sb, unsigned voff, unsigned ldsaddr){
  asm volatile("s_mov_b32 m0, %2\n\ts_mov_b64 exec, 0xffff\n\tglobal_load_lds_dwordx4 %0, %1\n\ts_mov_b64 exec, -1"::"v"(voff),"s"(sb),"s"(ldsaddr):"memory"); }
DI void glds16v(const char* vp, unsigned ldsaddr){
  asm volatile("s_mov_b32 m0, %1\n\ts_nop 0\n\tglobal_load_lds_dwordx4 %0, off"::"v"(vp),"s"(ldsaddr):"memory"); }
DI void gld(u32x4& a,unsigned voff,const char* sb){ asm volatile("global_load_dwordx4 %0, %1, %2":"=&v"(a):"v"(voff),"s"(sb):"memory"); }
DI void gldc(u32x4& a,unsigned voff,const char* sb){ asm volatile("global_load_dwordx4 %0, %1, %2 sc1":"=&v"(a):"v"(voff),"s"(sb):"memory"); }
DI void gstc(const u32x4& a,unsigned voff,char* sb){ asm volatile("global_store_dwordx4 %1, %0, %2 sc1\n\ts_nop 1"::"v"(a),"v"(voff),"s"(sb):"memory"); }
DI void gstc2(const u32x2& a,unsigned voff,char* sb){ asm volatile("global_store_dwordx2 %1, %0, %2 sc1"::"v"(a),"v"(voff),"s"(sb):"memory"); }
DI void gstc1(unsigned a,unsigned voff,char* sb){ asm volatile("global_store_dword %1, %0, %2 sc1"::"v"(a),"v"(voff),"s"(sb):"memory"); }
DI void gld1(unsigned& a,unsigned voff,const char* sb){ asm volatile("global_load_dword %0, %1, %2":"=&v"(a):"v"(voff),"s"(sb):"memory"); }
DI void gld2c(u32x2& a,unsigned voff,const char* sb){ asm volatile("global_load_dwordx2 %0, %1, %2 sc1":"=&v"(a):"v"(voff),"s"(sb):"memory"); }
template<int ST> DI void gld4m(unsigned* a,unsigned voff,const char* sb,unsigned m){
  asm volatile("s_cmp_eq_u32 %6, 0\n\ts_cbranch_scc1 .Lskip%=\n\tglobal_load_dword %0, %4, %5 sc1\n\tglobal_load_dword %1, %4, %5 offset:%7 sc1\n\tglobal_load_dword %2, %4, %5 offset:%8 sc1\n\tglobal_load_dword %3, %4, %5 offset:%9 sc1\n.Lskip%=:"
    :"=&v"(a[0]),"=&v"(a[1]),"=&v"(a[2]),"=&v"(a[3]):"v"(voff),"s"(sb),"s"(m),"n"(ST),"n"(2*ST),"n"(3*ST):"memory","scc"); }
template<int N> DI void wait_vm4m(unsigned* a,unsigned m){
  asm volatile("s_cmp_eq_u32 %4, 0\n\ts_cbranch_scc1 .Lskip%=\n\ts_waitcnt vmcnt(%5)\n.Lskip%=:":"+v"(a[0]),"+v"(a[1]),"+v"(a[2]),"+v"(a[3]):"s"(m),"n"(N):"memory","scc"); }
DI void gst4m(const u32x2& a,const u32x2& b,const u32x2& c,const u32x2& d,unsigned voff,char* sb,unsigned m){
  asm volatile("s_cmp_eq_u32 %6, 0\n\ts_cbranch_scc1 .Lskip%=\n\tglobal_store_dwordx2 %4, %0, %5 sc1\n\tglobal_store_dwordx2 %4, %1, %5 offset:512 sc1\n\tglobal_store_dwordx2 %4, %2, %5 offset:1024 sc1\n\tglobal_store_dwordx2 %4, %3, %5 offset:1536 sc1\n.Lskip%=:"
    ::"v"(a),"v"(b),"v"(c),"v"(d),"v"(voff),"s"(sb),"s"(m):"memory","scc"); }
template<class T> DI void pin(T& a){ asm volatile("":"+v"(a)); }
template<int N> DI void wait_vm4(u32x4& a,u32x4& b,u32x4& c,u32x4& d){ asm volatile("s_waitcnt vmcnt(%4)":"+v"(a),"+v"(b),"+v"(c),"+v"(d):"n"(N):"memory"); }
template<int N> DI void wait_vm3(u32x4& a,u32x4& b,u32x4& c){ asm volatile("s_waitcnt vmcnt(%3)":"+v"(a),"+v"(b),"+v"(c):"n"(N):"memory"); }
template<int N> DI void wait_vm2(u32x4& a,u32x4& b){ asm volatile("s_waitcnt vmcnt(%2)":"+v"(a),"+v"(b):"n"(N):"memory"); }
template<int C> DI float dppf(float v){ return __uint_as_float(__builtin_amdgcn_update_dpp(0u,__float_as_uint(v),C,0xf,0xf,true)); }
DI float sum8(float v){ v+=dppf<0xB1>(v); v+=dppf<0x4E>(v); v+=dppf<0x141>(v); return v; }
DI float xsum32(float v){ const auto r=__builtin_amdgcn_permlane32_swap(__float_as_uint(v),__float_as_uint(v),false,false); return __uint_as_float(r[0])+__uint_as_float(r[1]); }
DI bf16x8 pack8(const f32x16& c,int o){ u32x4 r={pk2(c[o],c[o+1]),pk2(c[o+2],c[o+3]),pk2(c[o+4],c[o+5]),pk2(c[o+6],c[o+7])}; return BF8(r); }

constexpr size_t WS_FLAG=0;
constexpr size_t WS_A=32768, WS_Q=36864;
constexpr int    KVP=5632;
constexpr size_t WS_KVP=65536;
constexpr size_t WS_O=WS_KVP+256*KVP;
constexpr size_t WS_H=WS_O+(size_t)S*D*2;
constexpr size_t WS_ST=WS_H+(size_t)S*D*2;
constexpr size_t WS_CS=WS_ST+9*S*16;
constexpr size_t WS_X=WS_CS+32*3*32*16;
constexpr size_t WS_G=WS_X+64*16*9*1024;

DI void wait_progress(char* ws,int who,int n,unsigned base,unsigned want,bool serve=false){
  const int lane=lane_id();
  const GAS unsigned* p=(const GAS unsigned*)(ws+WS_FLAG)+(lane<n?who:0)*32;
  for(unsigned spins=0;spins<(1u<<18);spins++){
    const unsigned v=__hip_atomic_load(p,RLX_AGENT);
    if(serve){ unsigned qv[4]; unsigned long long av[4];
      #pragma unroll
      for(int i=0;i<4;i++){ qv[i]=__hip_atomic_load((const GAS unsigned*)(ws+WS_Q)+i*64+lane,RLX_AGENT); av[i]=__hip_atomic_load((const GAS unsigned long long*)(ws+WS_A)+i*64+lane,RLX_AGENT); }
      #pragma unroll
      for(int i=0;i<4;i++){ const unsigned long long a=((unsigned long long)base<<32)|qv[i]; if(av[i]!=a) __hip_atomic_store((GAS unsigned long long*)(ws+WS_A)+i*64+lane,a,RLX_AGENT); } }
    if(__all(lane>=n || v-base-want<8u-want)) break;
    __builtin_amdgcn_s_sleep(1);
  }
}
DI void publish(char* ws,int bx,unsigned v){ __hip_atomic_store((GAS unsigned*)(ws+WS_FLAG)+bx*32,v,RLX_AGENT); }

__global__ __launch_bounds__(512,2) void mega(const char* __restrict__ x,const char* __restrict__ wqkv,const char* __restrict__ wphi,const char* __restrict__ wo,
                                              const char* __restrict__ wg,const char* __restrict__ wu,const char* __restrict__ wd,char* __restrict__ ws,char* __restrict__ outp){
  extern __shared__ __attribute__((aligned(16))) char lds_[];
  LAS char* lds=(LAS char*)lds_; const unsigned lb=(unsigned)(size_t)lds;
  const int w=__builtin_amdgcn_readfirstlane(threadIdx.x>>6);
  const int bx=blockIdx.x, xcd=bx&7, ii=bx>>3;
  STAMP_DECL STAMP(0);
  const char* pc0; asm volatile("s_getpc_b64 %0":"=s"(pc0));
  unsigned base;
  {
    const int lane=lane_id(), hh=lane>>5, r=lane&31; const unsigned l16=lane*16;
    const int hg=4*(xcd&1)+(ii&3), tk=8*(xcd>>1)+(ii>>2), tg=w&1, hp=(w>>1)&1;
    constexpr int RB=73728, SL=5120, TI=114688, KVI=156672, RS=162832, BW=163832, PD=2;
    const int q=(lane&15)>>2, p=lane&3, g2=(lane>>4)&1, sg=2*g2+(p>>1), kw=w&3;
    const int rot=((ii>>2)+2*(ii&3))%9;
    auto ub=[&](int u){ const int v=u+rot; return wqkv+hg*144+(size_t)(v>=9?v-9:v)*(64*3456); };
    const unsigned ab=lb+RB+(8*hh+q)*320+72*hp+32*(p&1), aB_=lb+tg*36864+hh*512;
    const int tl=tg*32+r, sig=(((tl>>1)&1)<<2)|(tl&1)|(((tl>>2)&1)<<1);
    LAS char* trow=lds+TI+((2*hp+hh)*64+tl)*128;
    unsigned vk[5];
    auto mkvk=[&](){
      #pragma unroll
      for(int i=0;i<5;i++){ const unsigned c=i*64+lane, row=(c*205u)>>12, cc=c-row*20, t=cc>=10, c2=cc-10*t; vk[i]=(kw*16+row)*3456+1152*(1+t)+(c2<8?c2:8)*16; } };
    auto dma=[&](int u){
      #pragma unroll
      for(int i=0;i<5;i++) glds16(ub(u),vk[i],lb+RB+(4*(u&3)+kw)*SL+i*1024); };
    u32x4 pw[6];
    constexpr int PHS=1440;
    auto ldpw=[&](){ const unsigned b1=lb+KVI+kw*PHS+(hh*16+q)*40+32, b0=b1-32+8*sg;
      const unsigned mp=(((r>>2)&1)==hh)?~0u:0u, m0=hh?0u:mp, m1=hh?mp:0u;
      #pragma unroll
      for(int n=0;n<6;n++){ const unsigned b=(n<3?b0:b1)+(n%3)*320;
        const u32x2 lo=__builtin_bit_cast(u32x2,trd((LAS char*)(size_t)b));
        if(n%3<2){ const u32x2 hi=__builtin_bit_cast(u32x2,trd((LAS char*)(size_t)(b+160))); pw[n]=u32x4{lo[0]&(n%3?mp:m0),lo[1]&mp,hi[0]&mp,hi[1]&mp}; }
        else pw[n]=u32x4{lo[0]&mp,lo[1]&m1,0u,0u}; } };
    auto feat=[&](const f32x16& c0,const f32x16& c1,float rs,float* e1,float* e2,float& s1,float& s2){
      f32x16 p0={},p1={};
      { const bf16x8 b0=pack8(c0,0), b1=pack8(c0,8), b2=pack8(c1,0);
        p0=MFMA32(BF8(pw[0]),b0,p0); p1=MFMA32(BF8(pw[3]),b0,p1);
        p0=MFMA32(BF8(pw[1]),b1,p0); p1=MFMA32(BF8(pw[4]),b1,p1);
        p0=MFMA32(BF8(pw[2]),b2,p0); p1=MFMA32(BF8(pw[5]),b2,p1); }
      float pv[18];
      #pragma unroll
      for(int i=0;i<16;i++) pv[i]=p0[i];
      pv[16]=p1[0]; pv[17]=p1[1];
      float mx=pv[0],mn=pv[0];
      #pragma unroll
      for(int i=1;i<18;i++){ mx=fmaxf(mx,pv[i]); mn=fminf(mn,pv[i]); }
      const float L=1.44269504f*rs, a=-mx*L, b=mn*L; s1=0.f; s2=0.f;
      #pragma unroll
      for(int i=0;i<18;i++){ e1[i]=__builtin_amdgcn_exp2f(fmaf(pv[i],L,a)); e2[i]=__builtin_amdgcn_exp2f(fmaf(pv[i],-L,b)); s1+=e1[i]; s2+=e2[i]; } };
    u32x2 av; u32x4 xv[18]; unsigned cp;
    if(w<4){
      __builtin_amdgcn_s_setprio(3);
      gld2c(av,0u,ws+WS_A+bx*8);
      const char* xs=x+(size_t)(tk*64+w*16)*1152; const unsigned xo=(lane>>3)*1152+(lane&7)*16;
      #pragma unroll
      for(int i=0;i<18;i++){ const int c=i%9+rot; gld(xv[i],xo,xs+(c>=9?c-9:c)*128+(i/9)*9216); }
      __builtin_amdgcn_s_setprio(0);
    } else {
      { const unsigned o=(ii*4+kw)*8192+lane*128; gld1(cp,o<CODE_PF?o:0u,pc0); }
      mkvk(); dma(0); dma(1);
    }
    int w2=w; asm volatile("":"+s"(w2));
    unsigned aB[4];
    #pragma unroll
    for(int i=0;i<4;i++) aB[i]=aB_+(r^(2*i+hh))*16;
    if(w2<4){
      const unsigned mk=__builtin_amdgcn_readfirstlane((bx==0&&w==0)?~0u:0u);
      unsigned pg[4], qv[4], nb=0;
      gld4m<128>(pg,lane*512,ws+WS_FLAG,mk);
      PST(2,1);
      mkvk();
      unsigned vq[3];
      { unsigned c=w*64+lane, row=c/9; vq[0]=row*3456+(c-row*9)*16; c+=256; row=c/9; vq[1]=row*3456+(c-row*9)*16; c=512+16*w+(lane&15); row=c/9; vq[2]=row*3456+(c-row*9)*16; }
      auto ldq=[&](int kg,int gp){ const char* s=ub(kg); const unsigned L=lb+RB+gp*9216;
        glds16(s,vq[0],L+w*1024); glds16(s,vq[1],L+4096+w*1024); glds16q(s,vq[2],L+8192+w*256); };
      wait_vm<18>(); pin(av);
      PST(2,2);
      const unsigned req=av[0]+1u;
      if(w==0&&lane==0) gstc1(req,0u,ws+WS_Q+bx*4);
      auto var=[&](int h){ f32x4 ga={},gb={};
        #pragma unroll
        for(int i=0;i<9;i++){ const s16x4 lo=__builtin_bit_cast(s16x4,(u32x2{xv[9*h+i][0],xv[9*h+i][1]})), hi=__builtin_bit_cast(s16x4,(u32x2{xv[9*h+i][2],xv[9*h+i][3]}));
          ga=__builtin_amdgcn_mfma_f32_4x4x4bf16_1k(lo,lo,ga,0,0,0); gb=__builtin_amdgcn_mfma_f32_4x4x4bf16_1k(hi,hi,gb,0,0,0); }
        ga+=gb;
        const float qq=sum8((lane&2)?((lane&1)?ga[3]:ga[2]):((lane&1)?ga[1]:ga[0]));
        if((lane&7)==0) *(LAS float*)(lds+RS+(w*16+8*h+(lane>>3))*4)=__builtin_amdgcn_rsqf(qq*(1.f/D)+1e-5f); };
      { const bool dg=(lane>>4)==((lane&15)>>2); const unsigned o=dg?((lane&1)?0x3f800000u:0x00003f80u):0u, o0=(lane&2)?0u:o, o1=(lane&2)?o:0u, on=dg?0x3f803f80u:0u;
        const bf16x8 sa=BF8((u32x4{o0,o1,0u,0u})), sb=BF8((u32x4{0u,0u,o0,o1})), sq=BF8((u32x4{on,on,on,on}));
        static_for<0,2>([&](auto H){ constexpr int h=decltype(H)::value;
          f32x4 sm={};
          static_for<0,3>([&](auto G){ constexpr int g=9*h+3*decltype(G)::value; wait_vm3<15-g>(xv[g],xv[g+1],xv[g+2]);
            sm=MFMA16(sq,BF8(xv[g]),sm); sm=MFMA16(sq,BF8(xv[g+1]),sm); sm=MFMA16(sq,BF8(xv[g+2]),sm); SB(); });
          const float m=sum8(sm[0])*(-1.f/D); const f32x4 nm={m,m,m,m};
          if constexpr(h==1) PST(2,3);
          const int t5=(w&1)*16+8*h+(lane>>3), sub=lane&7;
          LAS char* dst=lds+((w>>1)*36+(sub>>1))*1024+((sub&1)*32+(t5^sub))*16;
          #pragma unroll
          for(int i=0;i<9;i++){ const f32x4 a=MFMA16(sa,BF8(xv[9*h+i]),nm), b=MFMA16(sb,BF8(xv[9*h+i]),nm);
            xv[9*h+i]=u32x4{pk2(a[0],a[1]),pk2(a[2],a[3]),pk2(b[0],b[1]),pk2(b[2],b[3])};
            *(LAS u32x4*)(dst+i*4096)=xv[9*h+i]; }
          if constexpr(h==0) var(0);
          SB(); }); }
      STAMP(1); PST(2,4);
      wait_lgkm0(); bar();
      PST(2,5);
      auto ask=[&](){ gld4m<256>(qv,lane*4,ws+WS_Q,mk); };
      auto echo=[&](){ gst4m(u32x2{qv[0],nb},u32x2{qv[1],nb},u32x2{qv[2],nb},u32x2{qv[3],nb},lane*8,ws+WS_A,mk); };
      var(1);
      pin(pg[0]); pin(pg[1]); pin(pg[2]); pin(pg[3]);
      if(mk){ nb=(__builtin_amdgcn_readfirstlane(pg[0])+8u)&~7u;
        while(__any((pg[0]^nb)<8u||(pg[1]^nb)<8u||(pg[2]^nb)<8u||(pg[3]^nb)<8u)) nb+=8u; }
      ask();
      PST(4,1); bar();
      dma(4);
      PST(4,2); bar(); PST(4,9);
      dma(5);
      PST(4,3); bar(); PST(4,10);
      dma(6); wait_vm<10>(); wait_vm4m<10>(qv,mk); echo(); ask();
      PST(4,4); bar(); PST(4,11);
      dma(7); wait_vm<10>();
      PST(4,5); bar(); PST(4,12);
      dma(8); wait_vm<10>();
      PST(4,6); bar(); PST(4,13);
      ldq(0,3); wait_vm<8>();
      PST(4,7); bar(); PST(4,14);
      wait_vm<3>(); wait_lgkm0();
      PST(4,8); bar(); PST(4,15);
      wait_vm4m<3>(qv,mk); echo();
      ldq(1,8);
      STAMP(2); PST(2,14);
      const float rs=*(LAS float*)(lds+RS+tl*4);
      ldpw();
      STAMP(3); PST(2,15);
      wait_lgkm0(); bar();
      gld2c(av,0u,ws+WS_A+bx*8);
      ldq(2,0); ldq(3,1); ldq(4,2);
      wait_vm<9>(); pin(av);
      if(w==0){
        if(bx!=0){ unsigned long long a=((unsigned long long)av[1]<<32)|av[0];
          for(unsigned spins=0;(unsigned)a!=req&&spins<(1u<<18);spins++){ __builtin_amdgcn_s_sleep(1); a=__hip_atomic_load((const GAS unsigned long long*)(ws+WS_A)+bx,RLX_AGENT); }
          nb=(unsigned)(a>>32); }
        *(LAS unsigned*)(lds+BW)=nb; }
      STAMP(4); PST(5,4);
      wait_lgkm0(); bar();
      PST(5,5);
      const unsigned aQ0=lb+RB+(8*hh+q)*144+72*hp+32*(p&1)+8*sg, aQ1=aQ0-8*sg+32, aQ0h=aQ0+36864, aQ1h=aQ1+36864;
      f32x16 c0={},c1={}; bf16x8 fa[PD+1],fb[PD+1],fc[PD+1];
      static_for<0,36+PD>([&](auto SS){ constexpr int s=decltype(SS)::value, m=s-PD, kg=s>>2, gp=(kg==0)?3:(kg==1)?8:(kg<5)?kg-2:kg-1, o=(gp&3)*9216+(gp==8?36864:0)+(s&3)*2304;
        if constexpr(s==8){ wait_vm<0>(); PST(5,6); bar(); STAMP(5); PST(5,7); }
        if constexpr(s<36){ fa[s%(PD+1)]=ldtr<576>(((gp>=4)?aQ0h:aQ0)+o); fc[s%(PD+1)]=ldtr<576>(((gp>=4)?aQ1h:aQ1)+o); fb[s%(PD+1)]=*(LAS bf16x8*)(size_t)(aB[s&3]+s*1024); SB(); }
        if constexpr(m>=0){ c0=MFMA32(fa[m%(PD+1)],fb[m%(PD+1)],c0); c1=MFMA32(fc[m%(PD+1)],fb[m%(PD+1)],c1); SB(); }
        if constexpr(s>=3&&s<7){ ldq(s+2,s+1); SB(); }
      });
      PST(5,8);
      base=__builtin_amdgcn_readfirstlane(*(LAS unsigned*)(lds+BW));
      unsigned ph[20]; float s1,s2;
      { float e1[18],e2[18]; feat(c0,c1,rs,e1,e2,s1,s2);
        const float a2=s1*__builtin_amdgcn_rcpf(s2);
        #pragma unroll
        for(int i=0;i<9;i++){ ph[i]=pk2(e1[2*i],e1[2*i+1]); ph[9+i]=pk2(e2[2*i]*a2,e2[2*i+1]*a2); }
        ph[18]=0u; ph[19]=0u; }
      const bool mine=((r>>2)&1)==hh;
      const LAS char* k0=lds+KVI+(mine?((2*hp+hh)*19+4*(r>>3)+(r&3))*80:6080);
      const LAS char* k1=lds+KVI+((mine&&r<8&&(r&3)<3)?((2*hp+hh)*19+16+(r&3))*80:6080);
      PST(5,9);
      bar();
      STAMP(6); PST(5,10);
      f32x16 o0={},o1={};
      #pragma unroll
      for(int kp=0;kp<5;kp++){ const bf16x8 b=BF8((u32x4{ph[4*kp],ph[4*kp+1],ph[4*kp+2],ph[4*kp+3]}));
        o0=MFMA32(*(const LAS bf16x8*)(k0+kp*16),b,o0); o1=MFMA32(*(const LAS bf16x8*)(k1+kp*16),b,o1); }
      const float id=__builtin_amdgcn_rcpf(fmaf(s1,1e-6f,o1[2]));
      char* dst=ws+WS_O+(size_t)(tk*2+tg)*36*1024; const int pr=2*hg+hp;
      gstc(u32x4{pk2(o0[0]*id,o0[1]*id),pk2(o0[2]*id,o0[3]*id),pk2(o0[4]*id,o0[5]*id),pk2(o0[6]*id,o0[7]*id)},l16,dst+(2*pr)*1024);
      gstc(u32x4{pk2(o0[8]*id,o0[9]*id),pk2(o0[10]*id,o0[11]*id),pk2(o0[12]*id,o0[13]*id),pk2(o0[14]*id,o0[15]*id)},l16,dst+(2*pr+1)*1024);
      { const unsigned t=pk2(o1[0]*id,o1[1]*id); const auto sw=__builtin_amdgcn_permlane32_swap(t,t,false,false);
        if(hh==0) gstc2(u32x2{sw[0],sw[1]},l16,dst+(32+(pr>>2))*1024+((pr>>1)&1)*512+(pr&1)*8); }
      PST(5,11);
      wait_vm<0>();
      PST(5,12);
    } else {
      PST(2,1);
      { const char* ps=wphi+(2*hg+hp)*1296;
        #pragma unroll
        for(int i=0;i<6;i++){ const unsigned c=(i<5?i*64:296)+lane, row=(c*205u)>>11, cc=c-row*10; glds4(ps,row*36+(cc<9?cc:8)*4,lb+KVI+kw*PHS+(i<5?i*256:1184)); }
      }
      dma(2); dma(3);
      PST(2,2);
      wait_vm<10>(); pin(cp); ldpw();
      PST(2,3);
      const unsigned aA0=ab+8*sg, aA1=ab+(sg==0?32:160+8*sg), aA2=ab+160+(sg==0?0:32), aA0h=aA0+8*SL, aA1h=aA1+8*SL, aA2h=aA2+8*SL;
      STAMP(1); PST(2,4);
      bar();
      PST(2,5);
      if(KPRIO) __builtin_amdgcn_s_setprio(KPRIO);
      f32x16 c0={},c1={},c2={}; bf16x8 fa0[PD+1],fa1[PD+1],fa2[PD+1],fb[PD+1];
      static_for<0,36+PD>([&](auto SS){ constexpr int s=decltype(SS)::value, m=s-PD;
        if constexpr(s>0&&s<36&&(s&3)==0){
          if constexpr(s==8) wait_vm<5>();
          if constexpr(s==12) wait_vm<0>();
          if constexpr(s!=28) wait_lgkm0();
          bar(); }
        if constexpr(s<36){ fb[s%(PD+1)]=*(LAS bf16x8*)(size_t)(aB[s&3]+s*1024); fa0[s%(PD+1)]=ldtr<1280>(((s&8)?aA0h:aA0)+(s&7)*SL); SB(); }
        if constexpr(m>=0){ c0=MFMA32(fa0[m%(PD+1)],fb[m%(PD+1)],c0); SB(); }
        if constexpr(s<36){ fa1[s%(PD+1)]=ldtr<1280>(((s&8)?aA1h:aA1)+(s&7)*SL); SB(); }
        if constexpr(m>=0){ c1=MFMA32(fa1[m%(PD+1)],fb[m%(PD+1)],c1); SB(); }
        if constexpr(s<36){ fa2[s%(PD+1)]=ldtr<1280>(((s&8)?aA2h:aA2)+(s&7)*SL); SB(); }
        if constexpr(m>=0){ c2=MFMA32(fa2[m%(PD+1)],fb[m%(PD+1)],c2); SB(); }
      });
      STAMP(2); PST(2,14);
      { const float rs=*(LAS float*)(lds+RS+tl*4);
        float e1[18],e2[18],s1,s2; feat(c0,c1,rs,e1,e2,s1,s2);
        *(LAS u32x4*)(trow+((5^sig)*16))=u32x4{pk2(c2[0],c2[1]),pk2(c2[2],c2[3]),pk2(c1[4],c1[5]),pk2(c1[6],c1[7])};
        *(LAS u32x4*)(trow+((7^sig)*16))=u32x4{pk2(c2[4],c2[5]),pk2(c2[6],c2[7]),pk2(__builtin_amdgcn_rcpf(rs),0.f),0u};
        *(LAS u32x4*)(trow+((6^sig)*16))=__builtin_bit_cast(u32x4,pack8(c1,8));
        const float a1=rs*__builtin_amdgcn_rcpf(s1), a2=rs*__builtin_amdgcn_rcpf(s2); unsigned ph[20];
        #pragma unroll
        for(int i=0;i<9;i++){ ph[i]=pk2(e1[2*i]*a1,e1[2*i+1]*a1); ph[9+i]=pk2(e2[2*i]*a2,e2[2*i+1]*a2); }
        ph[18]=0u; ph[19]=0u;
        #pragma unroll
        for(int i=0;i<5;i++) *(LAS u32x4*)(trow+((i^sig)*16))=u32x4{ph[4*i],ph[4*i+1],ph[4*i+2],ph[4*i+3]}; }
      STAMP(3); PST(2,15); PST(5,1);
      wait_lgkm0(); bar();
      PST(5,2);
      {
        f32x16 ka={},kb={}; bf16x8 a[4],a2[4],b[4];
        #pragma unroll
        for(int s=0;s<4;s++){ s16x4 ta[2],tb[2],tc[2];
          #pragma unroll
          for(int t=0;t<2;t++){ const int sx=((q>>1)<<2)|(q&1)|(t<<1);
            LAS char* rp=lds+TI+(kw*64+16*s+8*hh+4*t+q)*128+(p&1)*8;
            ta[t]=trd(rp+((sg^sx)*16)); tb[t]=trd(rp+((((5+sg)&7)^sx)*16)); tc[t]=trd(rp+(((4+sg)^sx)*16)); }
          a[s]=cat(ta[0],ta[1]); b[s]=cat(tb[0],tb[1]); a2[s]=cat(tc[0],tc[1]); }
        SB();
        #pragma unroll
        for(int s=0;s<4;s++){ ka=MFMA32(a[s],b[s],ka); kb=MFMA32(a2[s],b[s],kb); }
        const int rho=r-2*(kw&1), row=(r>=20)?r-2:rho;
        char* gd=ws+WS_KVP+(size_t)(hg*32+tk)*KVP+kw*1408;
        if(r==20 || (rho>=0&&rho<18)){ gstc(__builtin_bit_cast(u32x4,pack8(ka,0)),row*32+hh*16,gd); gstc(__builtin_bit_cast(u32x4,pack8(ka,8)),row*32+hh*16,gd+608); }
        if(hh==0 && (r==20 || r==21 || (rho>=0&&rho<18))) gstc2(r==21?u32x2{0u,0u}:u32x2{pk2(kb[0],kb[1]),pk2(kb[2],kb[3])},row*8,gd+1216);
        PST(5,3);
        wait_vm<0>(); }
      STAMP(4); PST(5,4);
      __builtin_amdgcn_s_setprio(0);
      bar();
      PST(5,5);
      base=__builtin_amdgcn_readfirstlane(*(LAS unsigned*)(lds+BW));
      unsigned cq=0;
      if(w==7){ if(lane==0) publish(ws,bx,base+1);
        wait_progress(ws,((lane&7)*4+(hg&3))*8+((lane>>3)&3)*2+(hg>>2),32,base,1,bx==0); }
      else { if(ii<18){ const int u=(xcd&1)*18+ii; gld1(cp,((u&3)*144+kw*48+(lane<48?lane:0))*1152,wo+(u>>2)*128); }
        if(kw<2) gld1(cq,((ii&3)*48+(lane<48?lane:0))*(F*2),(kw?wu:wg)+(8*(xcd>>1)+(ii>>2))*256);
        LAS u32x4* z=(LAS u32x4*)(lds+KVI)+kw*64+lane; z[0]=u32x4{0u,0u,0u,0u}; z[192]=u32x4{0u,0u,0u,0u}; if(kw==0&&lane==0) z[384]=u32x4{0u,0u,0u,0u}; }
      unsigned kd[3];
      #pragma unroll
      for(int c=0;c<3;c++){ const int g=2*(32*c+(lane&15)+16*(lane>>5))+((lane>>4)&1), ty=g>=76, sl=(g-76*ty)>>1;
        kd[c]=g<171?lb+KVI+kw*1520+(g<152?(sl>>1)*80+(4*ty+2*(g&1)+(sl&1))*8:(g-152)*80+64):0u; }
      PST(5,6);
      wait_lgkm0(); bar();
      STAMP(5); PST(5,7);
      {
        const char* pb=ws+WS_KVP+(size_t)hg*32*KVP+kw*1408; const int rt=ii&28;
        const unsigned g0=r*16+hh*KVP, g2_=(r<22?r:21)*16+hh*KVP+1024;
        u32x4 sel; { const unsigned o=(((lane>>3)^(lane>>4))&1)?0u:((lane&1)?0x3f800000u:0x00003f80u); const int j=(lane&7)>>1; sel=u32x4{j==0?o:0u,j==1?o:0u,j==2?o:0u,j==3?o:0u}; }
        u32x4 pv[8][3]; f32x4 ac[3]={};
        auto ld=[&](int i){ const char* s=pb+(size_t)((2*i+rt)&31)*KVP; gldc(pv[i&7][0],g0,s); gldc(pv[i&7][1],g0,s+512); gldc(pv[i&7][2],g2_,s); };
        #pragma unroll
        for(int i=0;i<8;i++) ld(i);
        static_for<0,16>([&](auto I){ constexpr int i=decltype(I)::value;
          wait_vm3<(i<9)?21:3*(15-i)>(pv[i&7][0],pv[i&7][1],pv[i&7][2]);
          ac[0]=MFMA16(BF8(sel),BF8(pv[i&7][0]),ac[0]); ac[1]=MFMA16(BF8(sel),BF8(pv[i&7][1]),ac[1]); ac[2]=MFMA16(BF8(sel),BF8(pv[i&7][2]),ac[2]); SB();
          if constexpr(i<8){ ld(i+8); SB(); } });
        PST(5,8);
        pin(cp); pin(cq);
        #pragma unroll
        for(int c=0;c<3;c++) if(kd[c]) *(LAS u32x2*)(size_t)kd[c]=u32x2{pk2(ac[c][0],ac[c][1]),pk2(ac[c][2],ac[c][3])}; }
      PST(5,9);
      wait_lgkm0(); bar();
      STAMP(6); PST(5,10);
      if(ii<18){
        const int np=((xcd&1)*18+ii)>>2; const unsigned co=((lane&7)^(((lane>>4)&1)<<2))*16;
        #pragma unroll
        for(int v=0;v<2;v++){ const int rl=8*(kw+4*v)+(lane>>3), hk=(rl>>3)&1, j=rl&7;
          #pragma unroll
          for(int kb=0;kb<9;kb++){ const int s0=kb*4+((kw+4*v)>>1), h2=s0>=18, s1=s0-18*h2+2*np, s=18*h2+(s1>=18?s1-18:s1);
            const int row=(s<32)?36*(s>>1)+8*(s&1)+18*hk+j:144*(s-32)+16+72*hk+36*(j>>2)+18*((j>>1)&1)+(j&1);
            glds16(wo+np*128,row*1152+co,lb+RB+kb*8192+(kw+4*v)*1024); } } }
    }
    bar();
    if(w==0 && lane==0) publish(ws,bx,base+2);
  }
  STAMP(7); PST(5,13);
  const char* gA=wg+(8*(xcd>>1)+(ii>>2))*256; const char* uA=wu+(8*(xcd>>1)+(ii>>2))*256;
  asm volatile("":"+s"(gA),"+s"(uA));
  auto p3pre=[&](){
    const int lane=lane_id();
    const unsigned voffS=(unsigned)((8*w+(lane>>3))*(F*2)+(((lane&7)^(((lane>>4)&1)<<2))*16));
    #pragma unroll
    for(int kb=0;kb<3;kb++){ glds16(gA+(size_t)kb*64*F*2,voffS,lb+kb*16384+w*1024); glds16(uA+(size_t)kb*64*F*2,voffS,lb+kb*16384+8192+w*1024); } };
  if(ii<18){
    const int lane=lane_id(), hh=lane>>5, r=lane&31; const unsigned l16=lane*16;
    const int u=(xcd&1)*18+ii, np=u>>2, tk=4*(xcd>>1)+(u&3), tg=w&3, kh=w>>2;
    if(w==0) wait_progress(ws,(((2*tk+((lane>>3)&1))&7)*4+(lane&3))*8+(xcd&6)+((lane>>2)&1),16,base,2,bx==0);
    PST(5,14);
    wait_vm<0>(); bar();
    STAMP(8); PST(5,15); PST(6,1);
    u32x4 ov[18], xr[2];
    { const char* os=ws+WS_O+((size_t)(tk*4+tg)*36+kh*18)*1024;
      #pragma unroll
      for(int i=0;i<18;i++){ const int s=i+2*np; gldc(ov[i],l16,os+(s>=18?s-18:s)*1024); } }
    const int q=(lane&15)>>2, p=lane&3, g2=(lane>>4)&1;
    const unsigned a0_=(8*hh+q)*128+(((2*g2+(p>>1))^((q>>1)<<2))*16)+(p&1)*8, a0=lb+73728+kh*18*2048+a0_, a1=lb+73728+kh*18*2048+(a0_^64);
    f32x16 c0={},c1={};
    static_for<0,9>([&](auto G){ constexpr int g=decltype(G)::value;
      const bf16x8 f0=ldtr<512>(a0+g*4096), f1=ldtr<512>(a1+g*4096), f2=ldtr<512>(a0+g*4096+2048), f3=ldtr<512>(a1+g*4096+2048);
      if constexpr(g==0) PST(6,2);
      if constexpr(g==1) PST(6,3);
      if constexpr(g==5){ const char* xs=x+((size_t)(tk*128+tg*32)*576+np*64+kh*32)*2; gld(xr[0],r*1152+hh*16,xs); gld(xr[1],r*1152+hh*16,xs+32); }
      wait_vm2<(g<5?16:18)-2*g>(ov[2*g],ov[2*g+1]);
      c0=MFMA32(f0,BF8(ov[2*g]),c0); c1=MFMA32(f1,BF8(ov[2*g]),c1); c0=MFMA32(f2,BF8(ov[2*g+1]),c0); c1=MFMA32(f3,BF8(ov[2*g+1]),c1); });
    PST(6,4);
    {
      LAS char* sx=lds+(w<6?49152+w*4096:147456+(w-6)*4096)+l16; LAS char* rx=lds+((w^4)<6?49152+(w^4)*4096:147456+((w^4)-6)*4096)+l16;
      #pragma unroll
      for(int i=0;i<4;i++) *(LAS u32x4*)(sx+i*1024)=kh?u32x4{__float_as_uint(c0[4*i]),__float_as_uint(c0[4*i+1]),__float_as_uint(c0[4*i+2]),__float_as_uint(c0[4*i+3])}
                                                       :u32x4{__float_as_uint(c1[4*i]),__float_as_uint(c1[4*i+1]),__float_as_uint(c1[4*i+2]),__float_as_uint(c1[4*i+3])};
      wait_lgkm0(); bar();
      #pragma unroll
      for(int i=0;i<4;i++){ const u32x4 v=*(LAS u32x4*)(rx+i*1024);
        #pragma unroll
        for(int e=0;e<4;e++) c0[4*i+e]=(kh?c1[4*i+e]:c0[4*i+e])+__uint_as_float(v[e]); } }
    PST(6,5);
    wait_vm2<0>(xr[0],xr[1]);
    PST(6,6);
    unsigned o[8]; float v[16], sm=0.f;
    #pragma unroll
    for(int i=0;i<2;i++){ const auto x0=__builtin_amdgcn_permlane32_swap(xr[i][0],xr[i][2],false,false), x1=__builtin_amdgcn_permlane32_swap(xr[i][1],xr[i][3],false,false);
      #pragma unroll
      for(int e=0;e<2;e++){ const int n=4*(2*i+e);
        o[4*i+2*e]=pk2(c0[n]+lo16(x0[e]),c0[n+1]+hi16(x0[e])); o[4*i+2*e+1]=pk2(c0[n+2]+lo16(x1[e]),c0[n+3]+hi16(x1[e])); } }
    char* dst=ws+WS_H+((size_t)(tk*4+tg)*36+np*4+kh*2)*1024;
    gstc(u32x4{o[0],o[1],o[2],o[3]},l16,dst); gstc(u32x4{o[4],o[5],o[6],o[7]},l16,dst+1024);
    #pragma unroll
    for(int i=0;i<8;i++){ v[2*i]=lo16(o[i]); v[2*i+1]=hi16(o[i]); sm+=v[2*i]+v[2*i+1]; }
    sm=xsum32(sm)*(1.f/32);
    float qq=0.f;
    #pragma unroll
    for(int i=0;i<16;i++){ const float d=v[i]-sm; qq+=d*d; }
    qq=xsum32(qq);
    if(hh==0) gstc2(u32x2{__float_as_uint(sm),__float_as_uint(qq)},r*16+kh*8,ws+WS_ST+((size_t)np*S+tk*128+tg*32)*16);
    PST(6,7);
    p3pre();
    PST(6,8);
    wait_vm<6>(); PST(6,9); bar();
    if(w==0 && lane==0) publish(ws,bx,base+3);
  } else if(ii<27){
    p3pre();
    const int lane=lane_id();
    const int u=(ii-18)*8+xcd, m=u/36, cb=u-m*36;
    const char* src=(m?wu:wg)+cb*256+(size_t)w*72*(F*2);
    float a[8]={0.f,0.f,0.f,0.f,0.f,0.f,0.f,0.f};
    u32x4 v[18];
    #pragma unroll
    for(int i=0;i<18;i++) gld(v[i],(lane>>4)*(F*2)+(lane&15)*16,src+(size_t)i*4*(F*2));
    wait_vm<0>();
    #pragma unroll
    for(int i=0;i<18;i++){ pin(v[i]);
      #pragma unroll
      for(int e=0;e<4;e++){ a[2*e]+=lo16(v[i][e]); a[2*e+1]+=hi16(v[i][e]); } }
    #pragma unroll
    for(int e=0;e<8;e++){ a[e]+=__shfl_xor(a[e],16); a[e]+=__shfl_xor(a[e],32); }
    LAS float* red=(LAS float*)(lds+65536);
    if(lane<16){
      #pragma unroll
      for(int e=0;e<8;e++) red[w*128+lane*8+e]=a[e]; }
    wait_lgkm0(); bar();
    if(w<2){ const int t_=w*64+lane; float s=0.f;
      #pragma unroll
      for(int i=0;i<8;i++) s+=red[i*128+t_];
      const unsigned hi=pk2(s,s)&0xffffu, lo=pk2(s-lo16(hi),0.f)&0xffffu;
      const int f=cb*128+t_; int j,t,l;
      if(f<4096){ j=f>>7; t=((f>>5)&3)*2+m; l=f&31; } else { j=(f-4096)>>4; t=8; l=(f&15)+16*m; }
      gstc1(hi|(lo<<16),(((j*3+(t>>2))*32+l)*4+(t&3))*4,ws+WS_CS); wait_vm<0>(); }
    bar();
    if(w==0 && lane==0) publish(ws,bx,base+3);
  } else {
    p3pre();
    const int lane=lane_id(), sk=xcd>>1;
    u32x4 v[16];
    #pragma unroll
    for(int i=0;i<16;i++){ const int o=((((xcd&1)*5+ii-27)*8+w)*16+i)*1024;
      gld(v[i],lane*16,wd+(o<1024*1152?(size_t)sk*1024*1152+o:(size_t)(4096+128*sk)*1152+(o-1024*1152))); }
    wait_vm<0>();
    #pragma unroll
    for(int i=0;i<16;i++) pin(v[i]);
  }
  STAMP(9); PST(6,10);
  {
    const int lane=lane_id();
    const int tt=4*(xcd&1)+(ii&3), j=8*(xcd>>1)+(ii>>2), T=tt*8+w;
    const unsigned voffS=(unsigned)((8*w+(lane>>3))*(F*2)+(((lane&7)^(((lane>>4)&1)<<2))*16));
    const unsigned xoff=lane*16; const char* xb=ws+WS_H+(size_t)T*36*1024;
    u32x4 x_[36], sp[5], cv[3];
    auto ldw=[&](int m,int kb,int ch,int ps){ glds16((m?uA:gA)+ch*128+(size_t)kb*64*F*2, voffS, lb+ps*16384+m*8192+w*1024); };
    if(w==0){
      int who; if(lane<18){ const int u=(lane>>1)*4+((2*tt+(lane&1))&3); who=(u%18)*8+(tt>>1)*2+u/18; } else { const int u=(lane&1)*36+((lane&2)?32+(j>>3):j); who=(18+(u>>3))*8+(u&7); }
      wait_progress(ws,who,22,base,3); }
    bar();
    STAMP(10); PST(6,11);
    #pragma unroll
    for(int i=0;i<8;i++) gldc(x_[i],xoff,xb+i*1024);
    { const unsigned l16=(lane&31)*16, so=l16+(lane>>5)*(4*S*16); const char* sb=ws+WS_ST+(size_t)T*32*16; const char* cs=ws+WS_CS+j*1536;
      gldc(sp[0],so,sb); gldc(sp[1],so,sb+S*16); gldc(sp[2],so,sb+2*S*16); gldc(sp[3],so,sb+3*S*16); gldc(sp[4],l16,sb+8*S*16);
      gldc(cv[0],l16,cs); gldc(cv[1],l16,cs+512); gldc(cv[2],l16,cs+1024); }
    #pragma unroll
    for(int i=8;i<12;i++) gldc(x_[i],xoff,xb+i*1024);
    if(w>=4) __builtin_amdgcn_s_setprio(1);
    const int q=(lane&15)>>2, p=lane&3, g2=(lane>>4)&1, hh=lane>>5;
    const unsigned a0_=(4*hh+q)*128+(((2*g2+(p>>1))^((q>>1)<<2))*16)+(p&1)*8, a0=lb+a0_, a1=lb+(a0_^64);
    const unsigned ac=lb+(4*hh+q)*64+(2*g2+(p>>1))*16+(p&1)*8;
    auto ldc=[&](int pi,int sl){ const unsigned l4=lane_id(); const int row0=pi*128+16*w-((pi==4&&w>=4)?64:0);
      const char* m=((l4&2)?wu:wg)+(size_t)(4096+16*j)*2+(size_t)row0*(F*2);
      glds16v(m+((l4>>2)*(F*2)+(l4&1)*16), lb+sl*8192+w*1024); };
    f32x16 acc[4]={};
    char* outb=ws+WS_G+(size_t)T*288*1024;
    constexpr int PD=4, NU=324;
    bf16x8 A[PD]; u32x4 eo, bm; float k1, iv, sm;
    auto unit=[](int u,int& kb,int& s,int& t){
      if(u<96){ kb=u>>4; s=(u>>2)&3; t=u&3; }
      else if(u<120){ int v=u-96; kb=6+(v>>3); s=(v>>1)&3; t=v&1; }
      else if(u<144){ int v=u-120; kb=6+(v>>3); s=(v>>1)&3; t=2+(v&1); }
      else if(u<216){ int v=u-144; kb=v>>3; s=(v>>1)&3; t=v&1; }
      else if(u<288){ int v=u-216; kb=v>>3; s=(v>>1)&3; t=2+(v&1); }
      else { int v=u-288; kb=v>>2; s=v&3; t=0; } };
    unsigned pb0=0,pb1=0;
    auto rd=[&](auto U){ constexpr int u=decltype(U)::value; int kb=0,s=0,t=0; unit(u,kb,s,t);
      if constexpr(u<288){ constexpr int kbc=(u<96)?(u>>4):(u<120)?6+((u-96)>>3):(u<144)?6+((u-120)>>3):(u<216)?((u-144)>>3):((u-216)>>3); constexpr int ps=(u<144)?kbc:(kbc+9)%10;
        if constexpr(u<96){ if constexpr((u&15)==0) pb0=addc<ps*16384>(a0); if constexpr((u&15)==2) pb1=addc<ps*16384>(a1); }
        else if constexpr((u&7)==0){ if constexpr(u<120||(u>=144&&u<216)) pb0=addc<ps*16384>(a0); else pb1=addc<ps*16384>(a1); }
        A[u%PD]=ldtr<1024>(((t&2)?pb1:pb0)+(t&1)*8192+s*2048); }
      else { constexpr int v=u-288, pi=v/8, sl=(pi<4)?16+pi:0;
        if constexpr((v&7)==0) pb0=addc<sl*8192>(ac);
        A[u%PD]=ldtr<512>(pb0+(v%8)*1024); } };
    float ea[2], eb[2], ep;
    auto job=[&](auto I,auto NE,const f32x16& g,const f32x16& u,int uo,int fs){ constexpr int i=decltype(I)::value, ne=decltype(NE)::value;
      constexpr int k=i/3, st=i%3, e=k-st;
      if constexpr(e>=0 && e<ne){
        if constexpr(st==0) ea[e&1]=__builtin_amdgcn_exp2f(g[e]*k1);
        if constexpr(st==1) eb[e&1]=__builtin_amdgcn_rcpf(fmaf(ea[e&1],iv,iv));
        if constexpr(st==2){ float v=opf(g[e]*u[uo+e]*eb[e&1]);
          if constexpr((e&1)==0) ep=v; else eo[(e>>1)&3]=pk2(ep,v);
          if constexpr((e&7)==7) gstc(eo,xoff,outb+(fs+(e>>3))*1024); } } };
    auto spread=[&](auto U,auto U0,auto L,const f32x16& g,const f32x16& uu,int fs){ constexpr int u=decltype(U)::value,u0=decltype(U0)::value,l=decltype(L)::value;
      if constexpr(u>=u0 && u<u0+l){ constexpr int b=(u-u0)*54/l, e=(u-u0+1)*54/l; static_for<b,e>([&](auto I){ job(I,IC(16),g,uu,0,fs); }); } };
    auto corr=[&](unsigned d,const f32x16& c){ u32x4 a={d,d&0xffffu,0u,0u}; return MFMA32(BF8(a),BF8(bm),c); };
    static_for<-PD,NU>([&](auto U){ constexpr int u=decltype(U)::value; constexpr int r=u+PD;
      if constexpr(r<=96 && (r&15)==0){ constexpr int kb=r>>4;
        constexpr int n=(kb==0)?16:(kb==1)?18:12;
        wait_vm4<n>(x_[4*kb],x_[4*kb+1],x_[4*kb+2],x_[4*kb+3]);
        bar(); }
      if constexpr(r==104){ wait_vm4<9>(x_[28],x_[29],x_[30],x_[31]); bar(); }
      if constexpr(r==112){ wait_vm4<6>(x_[32],x_[33],x_[34],x_[35]); bar(); }
      if constexpr(r==144){ wait_vm<3>(); bar(); STAMP(11); }
      if constexpr(r==144+40){ wait_vm<2>(); bar(); }
      if constexpr(r==288){ wait_vm<0>(); bar(); }
      if constexpr(u>=0){ int kb=0,s=0,t=0; unit(u,kb,s,t);
        if constexpr(u==144||u==145) acc[u-144]=corr(cv[1][u-144],f32x16{});
        if constexpr(u==216||u==217) acc[u-214]=corr(cv[1][2+u-216],f32x16{});
        if constexpr(u==288) acc[0]=corr(cv[2][0],f32x16{});
        if constexpr(u>=56&&u<60) acc[u-56]=corr(cv[0][u-56],acc[u-56]);
        acc[t]=MFMA32(A[u%PD],BF8(x_[u<288?4*kb+s:u-288]),acc[t]);
        SB(); }
      if constexpr(r<NU) rd(IC(r));
      if constexpr(r<96){ constexpr int kb=r>>4, o=r&15; constexpr int slot=(o==1)?0:(o==3)?1:(o==6)?2:(o==8)?3:(o==11)?4:(o==13)?5:-1;
        if constexpr(slot>=0){ if constexpr(slot<2) ldw(slot,kb+3,0,kb+3); else gldc(x_[4*(kb+3)+slot-2],xoff,xb+(4*(kb+3)+slot-2)*1024); } }
      else if constexpr(r<128){ constexpr int o=(r-96)&7; constexpr int slot=(o==1)?0:(o==3)?1:(o==6)?2:-1;
        if constexpr(slot>=0){ constexpr int n=((r-96)>>3)*3+slot, i=n>>1; ldw(n&1,i,1,i==0?9:i-1); } }
      if constexpr(r==130) ldw(0,6,1,5);
      if constexpr(r==133) ldw(1,6,1,5);
      if constexpr(r>=36 && r<48){
        if constexpr(r==36){ pin(sp[0]); pin(sp[1]); pin(sp[2]); pin(sp[3]); pin(sp[4]); pin(cv[0]); pin(cv[1]); pin(cv[2]); }
        if constexpr(r==37){ float a=__uint_as_float(hh?sp[4][2]:sp[4][0]);
          #pragma unroll
          for(int i=0;i<4;i++) a+=__uint_as_float(sp[i][0])+__uint_as_float(sp[i][2]);
          sm=xsum32(a)*(1.f/18); }
        if constexpr(r==41){ float d=__uint_as_float(hh?sp[4][2]:sp[4][0])-sm; float a=__uint_as_float(hh?sp[4][3]:sp[4][1]), b=d*d;
          #pragma unroll
          for(int i=0;i<8;i++){ d=__uint_as_float(sp[i>>1][(i&1)*2])-sm; a+=__uint_as_float(sp[i>>1][(i&1)*2+1]); b=fmaf(d,d,b); }
          a=fmaf(b,32.f,a);
          iv=xsum32(a)*(1.f/D)+1e-5f; }
        if constexpr(r==46){ k1=__builtin_amdgcn_rsqf(iv)*-1.44269504f;
          const unsigned mh=pk2(-sm,-sm); const float ml=-sm-lo16(mh);
          bm=u32x4{hh?0u:mh, hh?0u:(pk2(ml,ml)&0xffffu), 0u, 0u}; } }
      if constexpr(r==144+1) ldw(0,7,1,6);
      if constexpr(r==144+4) ldw(1,7,1,6);
      if constexpr(r==144+7) ldw(0,8,1,7);
      if constexpr(r==144+10) ldw(1,8,1,7);
      if constexpr(r==144+42) ldc(0,16);
      if constexpr(r==144+46) ldc(1,17);
      if constexpr(u==216+15){ SB(); bar(); }
      if constexpr(u==216+16) ldc(2,18);
      if constexpr(u==216+19) ldc(3,19);
      if constexpr(u==216+22) ldc(4,0);
      spread(U,IC(121),IC(23),acc[0],acc[1],8*j);
      spread(U,IC(146),IC(34),acc[2],acc[3],8*j+2);
      spread(U,IC(218),IC(66),acc[0],acc[1],8*j+4);
      spread(U,IC(290),IC(34),acc[2],acc[3],8*j+6);
      SB();
    });
    static_for<0,30>([&](auto I){ job(I,IC(8),acc[0],acc[0],8,256+j); });
    __builtin_amdgcn_s_setprio(0);
    wait_vm<0>(); bar();
    if(w==0 && lane==0) publish(ws,bx,base+5);
  }
  STAMP(12);
  {
    const int lane=lane_id(); const unsigned l16=lane*16;
    const int sk=xcd>>1, tt=8*(xcd&1)+(ii&7), ng=ii>>3, tg=w&3, kh=w>>2;
    constexpr int ST=18432, LA=4;
    const unsigned voffW=(unsigned)((8*w+(lane>>3))*(D*2)+(((lane&7)^(((lane>>4)&1)<<2))*16));
    const unsigned voffQ=(unsigned)((8*w+((lane&15)>>1))*(D*2)+(lane&1)*16+1024+ng*32);
    const char* hb=ws+WS_G+((size_t)(tt*4+tg)*288+2*kh)*1024;
    u32x4 hv[LA][2];
    auto f0of=[&](int kb){ return (kb<16)?1024*sk+64*kb:4096+128*sk+64*(kb-16); };
    auto ldW=[&](int kb,int pc){ const unsigned L=lb+(kb&7)*ST; const char* wsrc=wd+(size_t)f0of(kb)*D*2;
      if(pc==0) glds16(wsrc+ng*256,voffW,L+w*1024);
      if(pc==1) glds16(wsrc+ng*256+128,voffW,L+8192+w*1024);
      if(pc==2) glds16q(wsrc,voffQ,L+16384+w*256); };
    #pragma unroll
    for(int kb=0;kb<LA;kb++){ ldW(kb,0); ldW(kb,1); ldW(kb,2); }
    if(w==0) wait_progress(ws,((lane&7)*4+((tt>>1)&3))*8+xcd,8,base,5);
    bar();
    STAMP(13);
    #pragma unroll
    for(int kb=0;kb<LA;kb++){ gldc(hv[kb][0],l16,hb+(size_t)(f0of(kb)>>4)*1024); gldc(hv[kb][1],l16,hb+(size_t)(f0of(kb)>>4)*1024+1024); }
    if(w>=4) __builtin_amdgcn_s_setprio(1);
    const int q=(lane&15)>>2, p=lane&3, g2=(lane>>4)&1, hh=lane>>5;
    const unsigned a0_=(4*hh+q)*128+(((2*g2+(p>>1))^((q>>1)<<2))*16)+(p&1)*8, a0=lb+kh*4096+a0_, a1=lb+kh*4096+(a0_^64);
    const unsigned aq=lb+16384+kh*1024+(4*hh+q)*32+(p>>1)*16+(p&1)*8;
    f32x16 c[5]={};
    constexpr int PD=4, NU=180;
    bf16x8 A[PD]; u32x4 hc[2]; unsigned pb0=0,pb1=0,pbq=0;
    static_for<-PD,NU>([&](auto U){ constexpr int u=decltype(U)::value, r=u+PD;
      if constexpr(r<NU && r%10==0){ constexpr int kb=r/10;
        constexpr int n=(kb<LA)?(LA-1-kb)*2+kb*5:((17-kb<LA-1)?(17-kb):(LA-1))*5;
        wait_vm2<n>(hv[kb%LA][0],hv[kb%LA][1]); bar();
        pb0=addc<(kb&7)*ST>(a0); pb1=addc<(kb&7)*ST>(a1); pbq=addc<(kb&7)*ST>(aq); }
      if constexpr(u>=0){ constexpr int kb=u/10, s=(u%10)/5, t=u%5;
        if constexpr(u%10==0){ hc[0]=hv[kb%LA][0]; hc[1]=hv[kb%LA][1]; }
        c[t]=MFMA32(A[u%PD],BF8(hc[s]),c[t]); SB(); }
      if constexpr(r<NU){ constexpr int s=(r%10)/5, t=r%5;
        if constexpr(t<4) A[r%PD]=ldtr<1024>(((t&1)?pb1:pb0)+(t>>1)*8192+s*2048); else A[r%PD]=ldtr<256>(pbq+s*512); }
      if constexpr(u>=0 && u/10+LA<18){ constexpr int kb=u/10+LA, o=u%10;
        if constexpr(o==0) ldW(kb,0); if constexpr(o==2) ldW(kb,1); if constexpr(o==4) ldW(kb,2);
        if constexpr(o==1) gldc(hv[kb%LA][0],l16,hb+(size_t)(f0of(kb)>>4)*1024);
        if constexpr(o==3) gldc(hv[kb%LA][1],l16,hb+(size_t)(f0of(kb)>>4)*1024+1024); }
      SB();
    });
    __builtin_amdgcn_s_setprio(0);
    STAMP(14);
    bar();
    { LAS char* sx=lds+w*10240+l16; LAS char* rx=lds+(w^4)*10240+l16;
      auto put=[&](const f32x16& a,int g,int o){ *(LAS u32x4*)(sx+o*1024)=u32x4{__float_as_uint(a[4*g]),__float_as_uint(a[4*g+1]),__float_as_uint(a[4*g+2]),__float_as_uint(a[4*g+3])}; };
      auto get=[&](f32x16& a,int g,int o){ const u32x4 v=*(LAS u32x4*)(rx+o*1024);
        #pragma unroll
        for(int e=0;e<4;e++) a[4*g+e]+=__uint_as_float(v[e]); };
      if(kh){
        #pragma unroll
        for(int g=0;g<4;g++){ put(c[0],g,g); put(c[1],g,4+g); }
        put(c[4],0,8); put(c[4],1,9);
        c[0]=c[2]; c[1]=c[3];
      } else {
        #pragma unroll
        for(int g=0;g<4;g++){ put(c[2],g,g); put(c[3],g,4+g); } }
      wait_lgkm0(); bar();
      #pragma unroll
      for(int g=0;g<4;g++){ get(c[0],g,g); get(c[1],g,4+g); }
      if(!kh){ get(c[4],0,8); get(c[4],1,9); } }
    const int tile=tt*4+ng;
    char* xb=ws+WS_X+(size_t)(tile*4+tg)*4*9216;
    u32x4 rr[5];
    if(tg!=sk){ char* d=xb+sk*9216+kh*4096;
      gstc(__builtin_bit_cast(u32x4,pack8(c[0],0)),l16,d); gstc(__builtin_bit_cast(u32x4,pack8(c[0],8)),l16,d+1024);
      gstc(__builtin_bit_cast(u32x4,pack8(c[1],0)),l16,d+2048); gstc(__builtin_bit_cast(u32x4,pack8(c[1],8)),l16,d+3072);
      if(!kh) gstc(__builtin_bit_cast(u32x4,pack8(c[4],0)),l16,d+8192);
    } else { const char* hr=ws+WS_H+(size_t)(tt*4+tg)*36*1024;
      #pragma unroll
      for(int i=0;i<4;i++) gldc(rr[i],l16,hr+(ng*8+kh*4+i)*1024);
      gldc(rr[4],l16,hr+(32+ng)*1024); }
    wait_vm<0>(); bar();
    if(w==0 && lane==0) publish(ws,bx,base+6);
    if(tg==sk){
      wait_progress(ws,ii*8+(lane&3)*2+(xcd&1),4,base,6);
      STAMP(15);
      u32x4 pv[3][5];
      #pragma unroll
      for(int s=0;s<3;s++){ const char* d=xb+(s+(s>=sk?1:0))*9216;
        #pragma unroll
        for(int i=0;i<4;i++) gldc(pv[s][i],l16,d+kh*4096+i*1024);
        gldc(pv[s][4],l16,d+8192); }
      wait_vm<0>();
      #pragma unroll
      for(int i=0;i<5;i++){ pin(rr[i]); pin(pv[0][i]); pin(pv[1][i]); pin(pv[2][i]); }
      char* orow=outp+((size_t)((tt*4+tg)*32+(lane&31))*D+4*hh)*2;
      auto fin=[&](const f32x16& a,int half,int fi,int n0){
        float v[8];
        #pragma unroll
        for(int e=0;e<4;e++){
          v[2*e]=a[8*half+2*e]+lo16(rr[fi][e])+lo16(pv[0][fi][e])+lo16(pv[1][fi][e])+lo16(pv[2][fi][e]);
          v[2*e+1]=a[8*half+2*e+1]+hi16(rr[fi][e])+hi16(pv[0][fi][e])+hi16(pv[1][fi][e])+hi16(pv[2][fi][e]); }
        *(GAS u32x2*)(orow+n0*2)=u32x2{pk2(v[0],v[1]),pk2(v[2],v[3])}; *(GAS u32x2*)(orow+n0*2+16)=u32x2{pk2(v[4],v[5]),pk2(v[6],v[7])}; };
      const int nb=ng*128+kh*64;
      fin(c[0],0,0,nb); fin(c[0],1,1,nb+16); fin(c[1],0,2,nb+32); fin(c[1],1,3,nb+48);
      if(!kh) fin(c[4],0,4,512+ng*16);
    }
  }
  STAMP_OUT(0);
}

extern "C" void kernel_launch(void* const* d_in, const int* in_sizes, int n_in, void* d_out, int out_size, void* d_ws, size_t ws_size, hipStream_t stream){
  (void)hipFuncSetAttribute((const void*)mega,hipFuncAttributeMaxDynamicSharedMemorySize,163840);
  mega<<<256,512,163840,stream>>>((const char*)d_in[0],(const char*)d_in[1],(const char*)d_in[2],(const char*)d_in[3],(const char*)d_in[5],(const char*)d_in[6],(const char*)d_in[7],(char*)d_ws,(char*)d_out);
}
```

```cpp
#include <hip/hip_runtime.h>
#include <hip/hip_bf16.h>
#include <type_traits>
typedef unsigned short u16;
typedef __attribute__((ext_vector_type(4))) short s16x4;
typedef __attribute__((ext_vector_type(8))) __bf16 bf16x8;
typedef __attribute__((ext_vector_type(2))) __bf16 bf16x2;
typedef __attribute__((ext_vector_type(2))) float f32x2;
typedef __attribute__((ext_vector_type(2))) unsigned u32x2;
typedef __attribute__((ext_vector_type(4))) unsigned u32x4;
typedef __attribute__((ext_vector_type(4))) float f32x4;
typedef __attribute__((ext_vector_type(16))) float f32x16;
#define LAS __attribute__((address_space(3)))
#define GAS __attribute__((address_space(1)))
#define DI __device__ __forceinline__
#define RLX_AGENT __ATOMIC_RELAXED, __HIP_MEMORY_SCOPE_AGENT

#define STAMP(i)
#define PST(d,i)
#define STAMP_DECL
#define STAMP_OUT(k)
constexpr unsigned CODE_PF=65536;
#define KPRIO 0
constexpr int S=2048, D=576, F=4608;

template<int I,int N,class Fn> DI void static_for(Fn&& f){ if constexpr(I<N){ f(std::integral_constant<int,I>{}); static_for<I+1,N>(f);} }
#define IC(n) std::integral_constant<int,(n)>{}
#define SB() __builtin_amdgcn_sched_barrier(0)
#define MFMA32(a,b,c) __builtin_amdgcn_mfma_f32_32x32x16_bf16((a),(b),(c),0,0,0)
#define MFMA16(a,b,c) __builtin_amdgcn_mfma_f32_16x16x32_bf16((a),(b),(c),0,0,0)
#define BF8(v) __builtin_bit_cast(bf16x8,(v))
DI unsigned pk2(float a,float b){ f32x2 f={a,b}; return __builtin_bit_cast(unsigned,__builtin_convertvector(f,bf16x2)); }
DI float lo16(unsigned u){ return __uint_as_float(u<<16); }
DI float hi16(unsigned u){ return __uint_as_float(u&0xffff0000u); }
DI float opf(float v){ asm("":"+v"(v)); return v; }
DI int lane_id(){ int l; asm volatile("v_mbcnt_lo_u32_b32 %0, -1, 0\n\tv_mbcnt_hi_u32_b32 %0, -1, %0":"=v"(l)); return l; }
template<int N> DI void wait_vm(){ asm volatile("s_waitcnt vmcnt(%0)"::"n"(N):"memory"); }
DI void wait_lgkm0(){ asm volatile("s_waitcnt lgkmcnt(0)":::"memory"); }
DI void bar(){ __builtin_amdgcn_s_barrier(); }
DI s16x4 trd(LAS char* p){ return __builtin_amdgcn_ds_read_tr16_b64_v4i16((LAS s16x4*)p); }
DI bf16x8 cat(s16x4 a,s16x4 b){ return BF8(__builtin_shufflevector(a,b,0,1,2,3,4,5,6,7)); }
template<int TS> DI bf16x8 ldtr(unsigned p){ return cat(trd((LAS char*)(size_t)p),trd((LAS char*)(size_t)p+TS)); }
template<int C> DI unsigned addc(unsigned v){ unsigned r; asm volatile("v_add_u32 %0, %1, %2":"=v"(r):"n"(C),"v"(v)); return r; }
DI void glds16(const char* sb, unsigned voff, unsigned ldsaddr){
  asm volatile("s_mov_b32 m0, %2\n\ts_nop 0\n\tglobal_load_lds_dwordx4 %0, %1"::"v"(voff),"s"(sb),"s"(ldsaddr):"memory"); }
template<int C,int OFF,bool SC> DI void gldsx(const char* sb, unsigned voff, unsigned lw){
  if constexpr(SC) asm volatile("s_add_i32 m0, %2, %3\n\ts_nop 0\n\tglobal_load_lds_dwordx4 %0, %1 offset:%4 sc1"::"v"(voff),"s"(sb),"s"(lw),"n"(C-OFF),"n"(OFF):"memory","scc");
  else asm volatile("s_add_i32 m0, %2, %3\n\ts_nop 0\n\tglobal_load_lds_dwordx4 %0, %1 offset:%4"::"v"(voff),"s"(sb),"s"(lw),"n"(C-OFF),"n"(OFF):"memory","scc"); }
template<int L> DI unsigned addlane(unsigned tab,unsigned v){ unsigned r,t; asm volatile("v_readlane_b32 %1, %2, %4\n\ts_nop 1\n\tv_add_u32 %0, %1, %3":"=v"(r),"=&s"(t):"v"(tab),"v"(v),"n"(L)); return r; }
DI void glds4(const char* sb, unsigned voff, unsigned ldsaddr){
  asm volatile("s_mov_b32 m0, %2\n\ts_nop 0\n\tglobal_load_lds_dword %0, %1"::"v"(voff),"s"(sb),"s"(ldsaddr):"memory"); }
DI void glds16q(const char* sb, unsigned voff, unsigned ldsaddr){
  asm volatile("s_mov_b32 m0, %2\n\ts_mov_b64 exec, 0xffff\n\tglobal_load_lds_dwordx4 %0, %1\n\ts_mov_b64 exec, -1"::"v"(voff),"s"(sb),"s"(ldsaddr):"memory"); }
DI void glds16v(const char* vp, unsigned ldsaddr){
  asm volatile("s_mov_b32 m0, %1\n\ts_nop 0\n\tglobal_load_lds_dwordx4 %0, off"::"v"(vp),"s"(ldsaddr):"memory"); }
DI void gld(u32x4& a,unsigned voff,const char* sb){ asm volatile("global_load_dwordx4 %0, %1, %2":"=&v"(a):"v"(voff),"s"(sb):"memory"); }
DI void gldc(u32x4& a,unsigned voff,const char* sb){ asm volatile("global_load_dwordx4 %0, %1, %2 sc1":"=&v"(a):"v"(voff),"s"(sb):"memory"); }
DI void gstc(const u32x4& a,unsigned voff,char* sb){ asm volatile("global_store_dwordx4 %1, %0, %2 sc1\n\ts_nop 1"::"v"(a),"v"(voff),"s"(sb):"memory"); }
DI void gstc2(const u32x2& a,unsigned voff,char* sb){ asm volatile("global_store_dwordx2 %1, %0, %2 sc1"::"v"(a),"v"(voff),"s"(sb):"memory"); }
DI void gstc1(unsigned a,unsigned voff,char* sb){ asm volatile("global_store_dword %1, %0, %2 sc1"::"v"(a),"v"(voff),"s"(sb):"memory"); }
DI void gld1(unsigned& a,unsigned voff,const char* sb){ asm volatile("global_load_dword %0, %1, %2":"=&v"(a):"v"(voff),"s"(sb):"memory"); }
DI void gld1c(unsigned& a,unsigned voff,const char* sb){ asm volatile("global_load_dword %0, %1, %2 sc1":"=&v"(a):"v"(voff),"s"(sb):"memory"); }
DI void gld2c(u32x2& a,unsigned voff,const char* sb){ asm volatile("global_load_dwordx2 %0, %1, %2 sc1":"=&v"(a):"v"(voff),"s"(sb):"memory"); }
template<int ST> DI void gld4m(unsigned* a,unsigned voff,const char* sb,unsigned m){
  asm volatile("s_cmp_eq_u32 %6, 0\n\ts_cbranch_scc1 .Lskip%=\n\tglobal_load_dword %0, %4, %5 sc1\n\tglobal_load_dword %1, %4, %5 offset:%7 sc1\n\tglobal_load_dword %2, %4, %5 offset:%8 sc1\n\tglobal_load_dword %3, %4, %5 offset:%9 sc1\n.Lskip%=:"
    :"=&v"(a[0]),"=&v"(a[1]),"=&v"(a[2]),"=&v"(a[3]):"v"(voff),"s"(sb),"s"(m),"n"(ST),"n"(2*ST),"n"(3*ST):"memory","scc"); }
template<int N> DI void wait_vm4m(unsigned* a,unsigned m){
  asm volatile("s_cmp_eq_u32 %4, 0\n\ts_cbranch_scc1 .Lskip%=\n\ts_waitcnt vmcnt(%5)\n.Lskip%=:":"+v"(a[0]),"+v"(a[1]),"+v"(a[2]),"+v"(a[3]):"s"(m),"n"(N):"memory","scc"); }
DI void gst4m(const u32x2& a,const u32x2& b,const u32x2& c,const u32x2& d,unsigned voff,char* sb,unsigned m){
  asm volatile("s_cmp_eq_u32 %6, 0\n\ts_cbranch_scc1 .Lskip%=\n\tglobal_store_dwordx2 %4, %0, %5 sc1\n\tglobal_store_dwordx2 %4, %1, %5 offset:512 sc1\n\tglobal_store_dwordx2 %4, %2, %5 offset:1024 sc1\n\tglobal_store_dwordx2 %4, %3, %5 offset:1536 sc1\n.Lskip%=:"
    ::"v"(a),"v"(b),"v"(c),"v"(d),"v"(voff),"s"(sb),"s"(m):"memory","scc"); }
template<class T> DI void pin(T& a){ asm volatile("":"+v"(a)); }
template<int N> DI void wait_vm4(u32x4& a,u32x4& b,u32x4& c,u32x4& d){ asm volatile("s_waitcnt vmcnt(%4)":"+v"(a),"+v"(b),"+v"(c),"+v"(d):"n"(N):"memory"); }
template<int N> DI void wait_vm3(u32x4& a,u32x4& b,u32x4& c){ asm volatile("s_waitcnt vmcnt(%3)":"+v"(a),"+v"(b),"+v"(c):"n"(N):"memory"); }
template<int N> DI void wait_vm2(u32x4& a,u32x4& b){ asm volatile("s_waitcnt vmcnt(%2)":"+v"(a),"+v"(b):"n"(N):"memory"); }
template<int C> DI float dppf(float v){ return __uint_as_float(__builtin_amdgcn_update_dpp(0u,__float_as_uint(v),C,0xf,0xf,true)); }
DI float sum8(float v){ v+=dppf<0xB1>(v); v+=dppf<0x4E>(v); v+=dppf<0x141>(v); return v; }
DI float xsum32(float v){ const auto r=__builtin_amdgcn_permlane32_swap(__float_as_uint(v),__float_as_uint(v),false,false); return __uint_as_float(r[0])+__uint_as_float(r[1]); }
DI bf16x8 pack8(const f32x16& c,int o){ u32x4 r={pk2(c[o],c[o+1]),pk2(c[o+2],c[o+3]),pk2(c[o+4],c[o+5]),pk2(c[o+6],c[o+7])}; return BF8(r); }

constexpr size_t WS_FLAG=0;
constexpr size_t WS_A=32768, WS_Q=36864;
constexpr int    KVP=5632;
constexpr size_t WS_KVP=65536;
constexpr size_t WS_O=WS_KVP+256*KVP;
constexpr size_t WS_H=WS_O+(size_t)S*D*2;
constexpr size_t WS_ST=WS_H+(size_t)S*D*2;
constexpr size_t WS_CS=WS_ST+9*S*16;
constexpr size_t WS_X=WS_CS+32*3*32*16;
constexpr size_t WS_R=WS_X+64*16*8192;
constexpr size_t WS_G=WS_R+16*256*1024;

DI void wait_progress(char* ws,int who,int n,unsigned base,unsigned want,bool serve=false){
  const int lane=lane_id();
  const GAS unsigned* p=(const GAS unsigned*)(ws+WS_FLAG)+(lane<n?who:0)*32;
  for(unsigned spins=0;spins<(1u<<18);spins++){
    const unsigned v=__hip_atomic_load(p,RLX_AGENT);
    if(serve){ unsigned qv[4]; unsigned long long av[4];
      #pragma unroll
      for(int i=0;i<4;i++){ qv[i]=__hip_atomic_load((const GAS unsigned*)(ws+WS_Q)+i*64+lane,RLX_AGENT); av[i]=__hip_atomic_load((const GAS unsigned long long*)(ws+WS_A)+i*64+lane,RLX_AGENT); }
      #pragma unroll
      for(int i=0;i<4;i++){ const unsigned long long a=((unsigned long long)base<<32)|qv[i]; if(av[i]!=a) __hip_atomic_store((GAS unsigned long long*)(ws+WS_A)+i*64+lane,a,RLX_AGENT); } }
    if(__all(lane>=n || v-base-want<8u-want)) break;
    __builtin_amdgcn_s_sleep(1);
  }
}
DI void publish(char* ws,int bx,unsigned v){ __hip_atomic_store((GAS unsigned*)(ws+WS_FLAG)+bx*32,v,RLX_AGENT); }

constexpr int slot4(int p){ return (p&3)==3?114688:8192+(p&3)*32768; }
constexpr int st0(int p){ return 8*p+4*((p+3)>>2); }
constexpr int stg(int u){ int p=0; while(p<17&&st0(p+1)<=u) p++; return p; }

__global__ __launch_bounds__(512,2) void mega(const char* __restrict__ x,const char* __restrict__ wqkv,const char* __restrict__ wphi,const char* __restrict__ wo,
                                              const char* __restrict__ wg,const char* __restrict__ wu,const char* __restrict__ wd,char* __restrict__ ws,char* __restrict__ outp){
  extern __shared__ __attribute__((aligned(16))) char lds_[];
  LAS char* lds=(LAS char*)lds_; const unsigned lb=(unsigned)(size_t)lds;
  const int w=__builtin_amdgcn_readfirstlane(threadIdx.x>>6);
  const int bx=blockIdx.x, xcd=bx&7, ii=bx>>3;
  STAMP_DECL STAMP(0);
  const char* pc0; asm volatile("s_getpc_b64 %0":"=s"(pc0));
  unsigned base;
  {
    const int lane=lane_id(), hh=lane>>5, r=lane&31; const unsigned l16=lane*16;
    const int hg=4*(xcd&1)+(ii&3), tk=8*(xcd>>1)+(ii>>2), tg=w&1, hp=(w>>1)&1;
    constexpr int RB=73728, SL=5120, TI=114688, KVI=156672, RS=162832, BW=163832, PD=2;
    const int q=(lane&15)>>2, p=lane&3, g2=(lane>>4)&1, sg=2*g2+(p>>1), kw=w&3;
    const int rot=((ii>>2)+2*(ii&3))%9;
    auto ub=[&](int u){ const int v=u+rot; return wqkv+hg*144+(size_t)(v>=9?v-9:v)*(64*3456); };
    const unsigned ab=lb+RB+(8*hh+q)*320+72*hp+32*(p&1), aB_=lb+tg*36864+hh*512;
    const int tl=tg*32+r, sig=(((tl>>1)&1)<<2)|(tl&1)|(((tl>>2)&1)<<1);
    LAS char* trow=lds+TI+((2*hp+hh)*64+tl)*128;
    unsigned vk[5];
    auto mkvk=[&](){
      #pragma unroll
      for(int i=0;i<5;i++){ const unsigned c=i*64+lane, row=(c*205u)>>12, cc=c-row*20, t=cc>=10, c2=cc-10*t; vk[i]=(kw*16+row)*3456+1152*(1+t)+(c2<8?c2:8)*16; } };
    auto dma=[&](int u){
      #pragma unroll
      for(int i=0;i<5;i++) glds16(ub(u),vk[i],lb+RB+(4*(u&3)+kw)*SL+i*1024); };
    u32x4 pw[6];
    constexpr int PHS=1440;
    auto ldpw=[&](){ const unsigned b1=lb+KVI+kw*PHS+(hh*16+q)*40+32, b0=b1-32+8*sg;
      const unsigned mp=(((r>>2)&1)==hh)?~0u:0u, m0=hh?0u:mp, m1=hh?mp:0u;
      #pragma unroll
      for(int n=0;n<6;n++){ const unsigned b=(n<3?b0:b1)+(n%3)*320;
        const u32x2 lo=__builtin_bit_cast(u32x2,trd((LAS char*)(size_t)b));
        if(n%3<2){ const u32x2 hi=__builtin_bit_cast(u32x2,trd((LAS char*)(size_t)(b+160))); pw[n]=u32x4{lo[0]&(n%3?mp:m0),lo[1]&mp,hi[0]&mp,hi[1]&mp}; }
        else pw[n]=u32x4{lo[0]&mp,lo[1]&m1,0u,0u}; } };
    auto feat=[&](const f32x16& c0,const f32x16& c1,float rs,float* e1,float* e2,float& s1,float& s2){
      f32x16 p0={},p1={};
      { const bf16x8 b0=pack8(c0,0), b1=pack8(c0,8), b2=pack8(c1,0);
        p0=MFMA32(BF8(pw[0]),b0,p0); p1=MFMA32(BF8(pw[3]),b0,p1);
        p0=MFMA32(BF8(pw[1]),b1,p0); p1=MFMA32(BF8(pw[4]),b1,p1);
        p0=MFMA32(BF8(pw[2]),b2,p0); p1=MFMA32(BF8(pw[5]),b2,p1); }
      float pv[18];
      #pragma unroll
      for(int i=0;i<16;i++) pv[i]=p0[i];
      pv[16]=p1[0]; pv[17]=p1[1];
      float mx=pv[0],mn=pv[0];
      #pragma unroll
      for(int i=1;i<18;i++){ mx=fmaxf(mx,pv[i]); mn=fminf(mn,pv[i]); }
      const float L=1.44269504f*rs, a=-mx*L, b=mn*L; s1=0.f; s2=0.f;
      #pragma unroll
      for(int i=0;i<18;i++){ e1[i]=__builtin_amdgcn_exp2f(fmaf(pv[i],L,a)); e2[i]=__builtin_amdgcn_exp2f(fmaf(pv[i],-L,b)); s1+=e1[i]; s2+=e2[i]; } };
    u32x2 av; u32x4 xv[18]; unsigned cp;
    if(w<4){
      __builtin_amdgcn_s_setprio(3);
      gld2c(av,0u,ws+WS_A+bx*8);
      const char* xs=x+(size_t)(tk*64+w*16)*1152; const unsigned xo=(lane>>3)*1152+(lane&7)*16;
      #pragma unroll
      for(int i=0;i<18;i++){ const int c=i%9+rot; gld(xv[i],xo,xs+(c>=9?c-9:c)*128+(i/9)*9216); }
      __builtin_amdgcn_s_setprio(0);
    } else {
      { const unsigned o=(ii*4+kw)*8192+lane*128; gld1(cp,o<CODE_PF?o:0u,pc0); }
      mkvk(); dma(0); dma(1);
    }
    int w2=w; asm volatile("":"+s"(w2));
    unsigned aB[4];
    #pragma unroll
    for(int i=0;i<4;i++) aB[i]=aB_+(r^(2*i+hh))*16;
    if(w2<4){
      const unsigned mk=__builtin_amdgcn_readfirstlane((bx==0&&w==0)?~0u:0u);
      unsigned pg[4], qv[4], nb=0;
      gld4m<128>(pg,lane*512,ws+WS_FLAG,mk);
      PST(2,1);
      mkvk();
      unsigned vq[3];
      { unsigned c=w*64+lane, row=c/9; vq[0]=row*3456+(c-row*9)*16; c+=256; row=c/9; vq[1]=row*3456+(c-row*9)*16; c=512+16*w+(lane&15); row=c/9; vq[2]=row*3456+(c-row*9)*16; }
      auto ldq=[&](int kg,int gp){ const char* s=ub(kg); const unsigned L=lb+RB+gp*9216;
        glds16(s,vq[0],L+w*1024); glds16(s,vq[1],L+4096+w*1024); glds16q(s,vq[2],L+8192+w*256); };
      wait_vm<18>(); pin(av);
      PST(2,2);
      const unsigned req=av[0]+1u;
      if(w==0&&lane==0) gstc1(req,0u,ws+WS_Q+bx*4);
      auto var=[&](int h){ f32x4 ga={},gb={};
        #pragma unroll
        for(int i=0;i<9;i++){ const s16x4 lo=__builtin_bit_cast(s16x4,(u32x2{xv[9*h+i][0],xv[9*h+i][1]})), hi=__builtin_bit_cast(s16x4,(u32x2{xv[9*h+i][2],xv[9*h+i][3]}));
          ga=__builtin_amdgcn_mfma_f32_4x4x4bf16_1k(lo,lo,ga,0,0,0); gb=__builtin_amdgcn_mfma_f32_4x4x4bf16_1k(hi,hi,gb,0,0,0); }
        ga+=gb;
        const float qq=sum8((lane&2)?((lane&1)?ga[3]:ga[2]):((lane&1)?ga[1]:ga[0]));
        if((lane&7)==0) *(LAS float*)(lds+RS+(w*16+8*h+(lane>>3))*4)=__builtin_amdgcn_rsqf(qq*(1.f/D)+1e-5f); };
      { const bool dg=(lane>>4)==((lane&15)>>2); const unsigned o=dg?((lane&1)?0x3f800000u:0x00003f80u):0u, o0=(lane&2)?0u:o, o1=(lane&2)?o:0u, on=dg?0x3f803f80u:0u;
        const bf16x8 sa=BF8((u32x4{o0,o1,0u,0u})), sb=BF8((u32x4{0u,0u,o0,o1})), sq=BF8((u32x4{on,on,on,on}));
        static_for<0,2>([&](auto H){ constexpr int h=decltype(H)::value;
          f32x4 sm={};
          static_for<0,3>([&](auto G){ constexpr int g=9*h+3*decltype(G)::value; wait_vm3<15-g>(xv[g],xv[g+1],xv[g+2]);
            sm=MFMA16(sq,BF8(xv[g]),sm); sm=MFMA16(sq,BF8(xv[g+1]),sm); sm=MFMA16(sq,BF8(xv[g+2]),sm); SB(); });
          const float m=sum8(sm[0])*(-1.f/D); const f32x4 nm={m,m,m,m};
          if constexpr(h==1) PST(2,3);
          const int t5=(w&1)*16+8*h+(lane>>3), sub=lane&7;
          LAS char* dst=lds+((w>>1)*36+(sub>>1))*1024+((sub&1)*32+(t5^sub))*16;
          #pragma unroll
          for(int i=0;i<9;i++){ const f32x4 a=MFMA16(sa,BF8(xv[9*h+i]),nm), b=MFMA16(sb,BF8(xv[9*h+i]),nm);
            xv[9*h+i]=u32x4{pk2(a[0],a[1]),pk2(a[2],a[3]),pk2(b[0],b[1]),pk2(b[2],b[3])};
            *(LAS u32x4*)(dst+i*4096)=xv[9*h+i]; }
          if constexpr(h==0) var(0);
          SB(); }); }
      STAMP(1); PST(2,4);
      wait_lgkm0(); bar();
      PST(2,5);
      auto ask=[&](){ gld4m<256>(qv,lane*4,ws+WS_Q,mk); };
      auto echo=[&](){ gst4m(u32x2{qv[0],nb},u32x2{qv[1],nb},u32x2{qv[2],nb},u32x2{qv[3],nb},lane*8,ws+WS_A,mk); };
      pin(pg[0]); pin(pg[1]); pin(pg[2]); pin(pg[3]);
      if(mk){ nb=(__builtin_amdgcn_readfirstlane(pg[0])+8u)&~7u;
        while(__any((pg[0]^nb)<8u||(pg[1]^nb)<8u||(pg[2]^nb)<8u||(pg[3]^nb)<8u)) nb+=8u; }
      ask();
      PST(4,1); bar();
      dma(4); var(1);
      PST(4,2); bar(); PST(4,9);
      dma(5);
      PST(4,3); bar(); PST(4,10);
      dma(6); wait_vm<10>(); wait_vm4m<10>(qv,mk); echo(); ask();
      PST(4,4); bar(); PST(4,11);
      dma(7); wait_vm<10>();
      PST(4,5); bar(); PST(4,12);
      dma(8); wait_vm<10>();
      unsigned ct[2];
      { const int c=w*81+lane, c2=c+(lane<17?64:0); const char* m=((xcd&1)?wu:wg)+(size_t)ii*18*(F*2);
        auto off=[&](int e){ const int row=e/18, l=e-row*18; return (unsigned)(row*(F*2)+(l<16?(xcd>>1)*2048+l*128:8192+(xcd>>1)*256+(l-16)*128)); };
        gld1(ct[0],off(c),m); gld1(ct[1],off(c2),m); }
      PST(4,6); bar(); PST(4,13);
      ldq(0,3); wait_vm<10>();
      PST(4,7); bar(); PST(4,14);
      wait_vm<5>(); wait_lgkm0();
      PST(4,8); bar(); PST(4,15);
      wait_vm4m<5>(qv,mk); echo();
      ldq(1,8);
      STAMP(2); PST(2,14);
      const float rs=*(LAS float*)(lds+RS+tl*4);
      ldpw();
      STAMP(3); PST(2,15);
      wait_lgkm0(); bar();
      gld2c(av,0u,ws+WS_A+bx*8);
      ldq(2,0); ldq(3,1); ldq(4,2);
      wait_vm<9>(); pin(av); pin(ct[0]); pin(ct[1]);
      if(w==0){
        if(bx!=0){ unsigned long long a=((unsigned long long)av[1]<<32)|av[0];
          for(unsigned spins=0;(unsigned)a!=req&&spins<(1u<<18);spins++){ __builtin_amdgcn_s_sleep(1); a=__hip_atomic_load((const GAS unsigned long long*)(ws+WS_A)+bx,RLX_AGENT); }
          nb=(unsigned)(a>>32); }
        *(LAS unsigned*)(lds+BW)=nb; }
      STAMP(4); PST(5,4);
      wait_lgkm0(); bar();
      PST(5,5);
      const unsigned aQ0=lb+RB+(8*hh+q)*144+72*hp+32*(p&1)+8*sg, aQ1=aQ0-8*sg+32, aQ0h=aQ0+36864, aQ1h=aQ1+36864;
      f32x16 c0={},c1={}; bf16x8 fa[PD+1],fb[PD+1],fc[PD+1];
      static_for<0,36+PD>([&](auto SS){ constexpr int s=decltype(SS)::value, m=s-PD, kg=s>>2, gp=(kg==0)?3:(kg==1)?8:(kg<5)?kg-2:kg-1, o=(gp&3)*9216+(gp==8?36864:0)+(s&3)*2304;
        if constexpr(s==8){ wait_vm<0>(); PST(5,6); bar(); STAMP(5); PST(5,7); }
        if constexpr(s<36){ fa[s%(PD+1)]=ldtr<576>(((gp>=4)?aQ0h:aQ0)+o); fc[s%(PD+1)]=ldtr<576>(((gp>=4)?aQ1h:aQ1)+o); fb[s%(PD+1)]=*(LAS bf16x8*)(size_t)(aB[s&3]+s*1024); SB(); }
        if constexpr(m>=0){ c0=MFMA32(fa[m%(PD+1)],fb[m%(PD+1)],c0); c1=MFMA32(fc[m%(PD+1)],fb[m%(PD+1)],c1); SB(); }
        if constexpr(s>=3&&s<7){ ldq(s+2,s+1); SB(); }
      });
      PST(5,8);
      base=__builtin_amdgcn_readfirstlane(*(LAS unsigned*)(lds+BW));
      unsigned ph[20]; float s1,s2;
      { float e1[18],e2[18]; feat(c0,c1,rs,e1,e2,s1,s2);
        const float a2=s1*__builtin_amdgcn_rcpf(s2);
        #pragma unroll
        for(int i=0;i<9;i++){ ph[i]=pk2(e1[2*i],e1[2*i+1]); ph[9+i]=pk2(e2[2*i]*a2,e2[2*i+1]*a2); }
        ph[18]=0u; ph[19]=0u; }
      const bool mine=((r>>2)&1)==hh;
      const LAS char* k0=lds+KVI+(mine?((2*hp+hh)*19+4*(r>>3)+(r&3))*80:6080);
      const LAS char* k1=lds+KVI+((mine&&r<8&&(r&3)<3)?((2*hp+hh)*19+16+(r&3))*80:6080);
      PST(5,9);
      bar();
      STAMP(6); PST(5,10);
      f32x16 o0={},o1={};
      #pragma unroll
      for(int kp=0;kp<5;kp++){ const bf16x8 b=BF8((u32x4{ph[4*kp],ph[4*kp+1],ph[4*kp+2],ph[4*kp+3]}));
        o0=MFMA32(*(const LAS bf16x8*)(k0+kp*16),b,o0); o1=MFMA32(*(const LAS bf16x8*)(k1+kp*16),b,o1); }
      const float id=__builtin_amdgcn_rcpf(fmaf(s1,1e-6f,o1[2]));
      char* dst=ws+WS_O+(size_t)(tk*2+tg)*36*1024; const int pr=2*hg+hp;
      gstc(u32x4{pk2(o0[0]*id,o0[1]*id),pk2(o0[2]*id,o0[3]*id),pk2(o0[4]*id,o0[5]*id),pk2(o0[6]*id,o0[7]*id)},l16,dst+(2*pr)*1024);
      gstc(u32x4{pk2(o0[8]*id,o0[9]*id),pk2(o0[10]*id,o0[11]*id),pk2(o0[12]*id,o0[13]*id),pk2(o0[14]*id,o0[15]*id)},l16,dst+(2*pr+1)*1024);
      { const unsigned t=pk2(o1[0]*id,o1[1]*id); const auto sw=__builtin_amdgcn_permlane32_swap(t,t,false,false);
        if(hh==0) gstc2(u32x2{sw[0],sw[1]},l16,dst+(32+(pr>>2))*1024+((pr>>1)&1)*512+(pr&1)*8); }
      PST(5,11);
      wait_vm<0>();
      PST(5,12);
    } else {
      PST(2,1);
      { const char* ps=wphi+(2*hg+hp)*1296;
        #pragma unroll
        for(int i=0;i<6;i++){ const unsigned c=(i<5?i*64:296)+lane, row=(c*205u)>>11, cc=c-row*10; glds4(ps,row*36+(cc<9?cc:8)*4,lb+KVI+kw*PHS+(i<5?i*256:1184)); }
      }
      dma(2); dma(3);
      PST(2,2);
      wait_vm<10>(); pin(cp); ldpw();
      PST(2,3);
      const unsigned aA0=ab+8*sg, aA1=ab+(sg==0?32:160+8*sg), aA2=ab+160+(sg==0?0:32), aA0h=aA0+8*SL, aA1h=aA1+8*SL, aA2h=aA2+8*SL;
      STAMP(1); PST(2,4);
      bar();
      PST(2,5);
      if(KPRIO) __builtin_amdgcn_s_setprio(KPRIO);
      f32x16 c0={},c1={},c2={}; bf16x8 fa0[PD+1],fa1[PD+1],fa2[PD+1],fb[PD+1];
      static_for<0,36+PD>([&](auto SS){ constexpr int s=decltype(SS)::value, m=s-PD;
        if constexpr(s>0&&s<36&&(s&3)==0){
          if constexpr(s==8) wait_vm<5>();
          if constexpr(s==12) wait_vm<0>();
          if constexpr(s!=28) wait_lgkm0();
          bar(); }
        if constexpr(s<36){ fb[s%(PD+1)]=*(LAS bf16x8*)(size_t)(aB[s&3]+s*1024); fa0[s%(PD+1)]=ldtr<1280>(((s&8)?aA0h:aA0)+(s&7)*SL); SB(); }
        if constexpr(m>=0){ c0=MFMA32(fa0[m%(PD+1)],fb[m%(PD+1)],c0); SB(); }
        if constexpr(s<36){ fa1[s%(PD+1)]=ldtr<1280>(((s&8)?aA1h:aA1)+(s&7)*SL); SB(); }
        if constexpr(m>=0){ c1=MFMA32(fa1[m%(PD+1)],fb[m%(PD+1)],c1); SB(); }
        if constexpr(s<36){ fa2[s%(PD+1)]=ldtr<1280>(((s&8)?aA2h:aA2)+(s&7)*SL); SB(); }
        if constexpr(m>=0){ c2=MFMA32(fa2[m%(PD+1)],fb[m%(PD+1)],c2); SB(); }
      });
      STAMP(2); PST(2,14);
      { const float rs=*(LAS float*)(lds+RS+tl*4);
        float e1[18],e2[18],s1,s2; feat(c0,c1,rs,e1,e2,s1,s2);
        *(LAS u32x4*)(trow+((5^sig)*16))=u32x4{pk2(c2[0],c2[1]),pk2(c2[2],c2[3]),pk2(c1[4],c1[5]),pk2(c1[6],c1[7])};
        *(LAS u32x4*)(trow+((7^sig)*16))=u32x4{pk2(c2[4],c2[5]),pk2(c2[6],c2[7]),pk2(__builtin_amdgcn_rcpf(rs),0.f),0u};
        *(LAS u32x4*)(trow+((6^sig)*16))=__builtin_bit_cast(u32x4,pack8(c1,8));
        const float a1=rs*__builtin_amdgcn_rcpf(s1), a2=rs*__builtin_amdgcn_rcpf(s2); unsigned ph[20];
        #pragma unroll
        for(int i=0;i<9;i++){ ph[i]=pk2(e1[2*i]*a1,e1[2*i+1]*a1); ph[9+i]=pk2(e2[2*i]*a2,e2[2*i+1]*a2); }
        ph[18]=0u; ph[19]=0u;
        #pragma unroll
        for(int i=0;i<5;i++) *(LAS u32x4*)(trow+((i^sig)*16))=u32x4{ph[4*i],ph[4*i+1],ph[4*i+2],ph[4*i+3]}; }
      STAMP(3); PST(2,15); PST(5,1);
      wait_lgkm0(); bar();
      PST(5,2);
      {
        f32x16 ka={},kb={}; bf16x8 a[4],a2[4],b[4];
        #pragma unroll
        for(int s=0;s<4;s++){ s16x4 ta[2],tb[2],tc[2];
          #pragma unroll
          for(int t=0;t<2;t++){ const int sx=((q>>1)<<2)|(q&1)|(t<<1);
            LAS char* rp=lds+TI+(kw*64+16*s+8*hh+4*t+q)*128+(p&1)*8;
            ta[t]=trd(rp+((sg^sx)*16)); tb[t]=trd(rp+((((5+sg)&7)^sx)*16)); tc[t]=trd(rp+(((4+sg)^sx)*16)); }
          a[s]=cat(ta[0],ta[1]); b[s]=cat(tb[0],tb[1]); a2[s]=cat(tc[0],tc[1]); }
        SB();
        #pragma unroll
        for(int s=0;s<4;s++){ ka=MFMA32(a[s],b[s],ka); kb=MFMA32(a2[s],b[s],kb); }
        const int rho=r-2*(kw&1), row=(r>=20)?r-2:rho;
        char* gd=ws+WS_KVP+(size_t)(hg*32+tk)*KVP+kw*1408;
        if(r==20 || (rho>=0&&rho<18)){ gstc(__builtin_bit_cast(u32x4,pack8(ka,0)),row*32+hh*16,gd); gstc(__builtin_bit_cast(u32x4,pack8(ka,8)),row*32+hh*16,gd+608); }
        if(hh==0 && (r==20 || r==21 || (rho>=0&&rho<18))) gstc2(r==21?u32x2{0u,0u}:u32x2{pk2(kb[0],kb[1]),pk2(kb[2],kb[3])},row*8,gd+1216);
        PST(5,3);
        wait_vm<0>(); }
      STAMP(4); PST(5,4);
      __builtin_amdgcn_s_setprio(0);
      bar();
      PST(5,5);
      base=__builtin_amdgcn_readfirstlane(*(LAS unsigned*)(lds+BW));
      unsigned cq=0;
      if(w==7){ if(lane==0) publish(ws,bx,base+1);
        wait_progress(ws,((lane&7)*4+(hg&3))*8+((lane>>3)&3)*2+(hg>>2),32,base,1,bx==0); }
      else { if(ii<18){ const int u=(xcd&1)*18+ii; gld1(cp,((u&3)*144+kw*48+(lane<48?lane:0))*1152,wo+(u>>2)*128); }
        if(kw<2) gld1(cq,((ii&3)*48+(lane<48?lane:0))*(F*2),(kw?wu:wg)+(8*(xcd>>1)+(ii>>2))*256);
        LAS u32x4* z=(LAS u32x4*)(lds+KVI)+kw*64+lane; z[0]=u32x4{0u,0u,0u,0u}; z[192]=u32x4{0u,0u,0u,0u}; if(kw==0&&lane==0) z[384]=u32x4{0u,0u,0u,0u}; }
      unsigned kd[3];
      #pragma unroll
      for(int c=0;c<3;c++){ const int g=2*(32*c+(lane&15)+16*(lane>>5))+((lane>>4)&1), ty=g>=76, sl=(g-76*ty)>>1;
        kd[c]=g<171?lb+KVI+kw*1520+(g<152?(sl>>1)*80+(4*ty+2*(g&1)+(sl&1))*8:(g-152)*80+64):0u; }
      PST(5,6);
      wait_lgkm0(); bar();
      STAMP(5); PST(5,7);
      {
        const char* pb=ws+WS_KVP+(size_t)hg*32*KVP+kw*1408; const int rt=ii&28;
        const unsigned g0=r*16+hh*KVP, g2_=(r<22?r:21)*16+hh*KVP+1024;
        u32x4 sel; { const unsigned o=(((lane>>3)^(lane>>4))&1)?0u:((lane&1)?0x3f800000u:0x00003f80u); const int j=(lane&7)>>1; sel=u32x4{j==0?o:0u,j==1?o:0u,j==2?o:0u,j==3?o:0u}; }
        u32x4 pv[8][3]; f32x4 ac[3]={};
        auto ld=[&](int i){ const char* s=pb+(size_t)((2*i+rt)&31)*KVP; gldc(pv[i&7][0],g0,s); gldc(pv[i&7][1],g0,s+512); gldc(pv[i&7][2],g2_,s); };
        #pragma unroll
        for(int i=0;i<8;i++) ld(i);
        static_for<0,16>([&](auto I){ constexpr int i=decltype(I)::value;
          wait_vm3<(i<9)?21:3*(15-i)>(pv[i&7][0],pv[i&7][1],pv[i&7][2]);
          ac[0]=MFMA16(BF8(sel),BF8(pv[i&7][0]),ac[0]); ac[1]=MFMA16(BF8(sel),BF8(pv[i&7][1]),ac[1]); ac[2]=MFMA16(BF8(sel),BF8(pv[i&7][2]),ac[2]); SB();
          if constexpr(i<8){ ld(i+8); SB(); } });
        PST(5,8);
        pin(cp); pin(cq);
        #pragma unroll
        for(int c=0;c<3;c++) if(kd[c]) *(LAS u32x2*)(size_t)kd[c]=u32x2{pk2(ac[c][0],ac[c][1]),pk2(ac[c][2],ac[c][3])}; }
      PST(5,9);
      wait_lgkm0(); bar();
      STAMP(6); PST(5,10);
      if(ii<18){
        const int np=((xcd&1)*18+ii)>>2; const unsigned co=((lane&7)^(((lane>>4)&1)<<2))*16;
        #pragma unroll
        for(int v=0;v<2;v++){ const int rl=8*(kw+4*v)+(lane>>3), hk=(rl>>3)&1, j=rl&7;
          #pragma unroll
          for(int kb=0;kb<9;kb++){ const int s0=kb*4+((kw+4*v)>>1), h2=s0>=18, s1=s0-18*h2+2*np, s=18*h2+(s1>=18?s1-18:s1);
            const int row=(s<32)?36*(s>>1)+8*(s&1)+18*hk+j:144*(s-32)+16+72*hk+36*(j>>2)+18*((j>>1)&1)+(j&1);
            glds16(wo+np*128,row*1152+co,lb+RB+kb*8192+(kw+4*v)*1024); } } }
    }
    bar();
    if(w==0 && lane==0) publish(ws,bx,base+2);
  }
  STAMP(7); PST(5,13);
  const char* gA=wg+(8*(xcd>>1)+(ii>>2))*256; const char* uA=wu+(8*(xcd>>1)+(ii>>2))*256;
  asm volatile("":"+s"(gA),"+s"(uA));
  auto p3pre=[&](){
    const int lane=lane_id();
    const unsigned voffS=(unsigned)((8*w+(lane>>3))*(F*2)+(((lane&7)^(((lane>>4)&1)<<2))*16));
    #pragma unroll
    for(int kb=0;kb<3;kb++){ glds16(gA+(size_t)kb*64*F*2,voffS,lb+kb*16384+w*1024); glds16(uA+(size_t)kb*64*F*2,voffS,lb+kb*16384+8192+w*1024); } };
  if(ii<18){
    const int lane=lane_id(), hh=lane>>5, r=lane&31; const unsigned l16=lane*16;
    const int u=(xcd&1)*18+ii, np=u>>2, tk=4*(xcd>>1)+(u&3), tg=w&3, kh=w>>2;
    if(w==0) wait_progress(ws,(((2*tk+((lane>>3)&1))&7)*4+(lane&3))*8+(xcd&6)+((lane>>2)&1),16,base,2,bx==0);
    PST(5,14);
    wait_vm<0>(); bar();
    STAMP(8); PST(5,15); PST(6,1);
    u32x4 ov[18], xr[2];
    const char* os=ws+WS_O+((size_t)(tk*4+tg)*36+kh*18)*1024;
    auto ldo=[&](int i){ const int s=i+2*np; gldc(ov[i],l16,os+(s>=18?s-18:s)*1024); };
    {
      #pragma unroll
      for(int i=0;i<8;i++) ldo(i); }
    const int q=(lane&15)>>2, p=lane&3, g2=(lane>>4)&1;
    const unsigned a0_=(8*hh+q)*128+(((2*g2+(p>>1))^((q>>1)<<2))*16)+(p&1)*8, a0=lb+73728+kh*18*2048+a0_, a1=lb+73728+kh*18*2048+(a0_^64);
    f32x16 c0={},c1={};
    static_for<0,9>([&](auto G){ constexpr int g=decltype(G)::value;
      const bf16x8 f0=ldtr<512>(a0+g*4096), f1=ldtr<512>(a1+g*4096), f2=ldtr<512>(a0+g*4096+2048), f3=ldtr<512>(a1+g*4096+2048);
      if constexpr(g==0) PST(6,2);
      if constexpr(g==1) PST(6,3);
      wait_vm2<(g<7?6:16+4-2*g-2)>(ov[2*g],ov[2*g+1]);
      c0=MFMA32(f0,BF8(ov[2*g]),c0); c1=MFMA32(f1,BF8(ov[2*g]),c1); SB();
      if constexpr(g<5){ ldo(2*g+8); ldo(2*g+9); }
      if constexpr(g==5){ const char* xs=x+((size_t)(tk*128+tg*32)*576+np*64+kh*32)*2; gld(xr[0],r*1152+hh*16,xs); gld(xr[1],r*1152+hh*16,xs+32); }
      c0=MFMA32(f2,BF8(ov[2*g+1]),c0); c1=MFMA32(f3,BF8(ov[2*g+1]),c1); SB(); });
    PST(6,4);
    {
      LAS char* sx=lds+(w<6?49152+w*4096:147456+(w-6)*4096)+l16; LAS char* rx=lds+((w^4)<6?49152+(w^4)*4096:147456+((w^4)-6)*4096)+l16;
      #pragma unroll
      for(int i=0;i<4;i++) *(LAS u32x4*)(sx+i*1024)=kh?u32x4{__float_as_uint(c0[4*i]),__float_as_uint(c0[4*i+1]),__float_as_uint(c0[4*i+2]),__float_as_uint(c0[4*i+3])}
                                                       :u32x4{__float_as_uint(c1[4*i]),__float_as_uint(c1[4*i+1]),__float_as_uint(c1[4*i+2]),__float_as_uint(c1[4*i+3])};
      wait_lgkm0(); bar();
      #pragma unroll
      for(int i=0;i<4;i++){ const u32x4 v=*(LAS u32x4*)(rx+i*1024);
        #pragma unroll
        for(int e=0;e<4;e++) c0[4*i+e]=(kh?c1[4*i+e]:c0[4*i+e])+__uint_as_float(v[e]); } }
    PST(6,5);
    wait_vm2<0>(xr[0],xr[1]);
    PST(6,6);
    unsigned o[8]; float v[16], sm=0.f;
    #pragma unroll
    for(int i=0;i<2;i++){ const auto x0=__builtin_amdgcn_permlane32_swap(xr[i][0],xr[i][2],false,false), x1=__builtin_amdgcn_permlane32_swap(xr[i][1],xr[i][3],false,false);
      #pragma unroll
      for(int e=0;e<2;e++){ const int n=4*(2*i+e);
        o[4*i+2*e]=pk2(c0[n]+lo16(x0[e]),c0[n+1]+hi16(x0[e])); o[4*i+2*e+1]=pk2(c0[n+2]+lo16(x1[e]),c0[n+3]+hi16(x1[e])); } }
    char* dst=ws+WS_H+((size_t)(tk*4+tg)*36+np*4+kh*2)*1024;
    gstc(u32x4{o[0],o[1],o[2],o[3]},l16,dst); gstc(u32x4{o[4],o[5],o[6],o[7]},l16,dst+1024);
    #pragma unroll
    for(int i=0;i<8;i++){ v[2*i]=lo16(o[i]); v[2*i+1]=hi16(o[i]); sm+=v[2*i]+v[2*i+1]; }
    sm=xsum32(sm)*(1.f/32);
    float qq=0.f;
    #pragma unroll
    for(int i=0;i<16;i++){ const float d=v[i]-sm; qq+=d*d; }
    qq=xsum32(qq);
    if(hh==0) gstc2(u32x2{__float_as_uint(sm),__float_as_uint(qq)},r*16+kh*8,ws+WS_ST+((size_t)np*S+tk*128+tg*32)*16);
    PST(6,7);
    wait_vm<0>(); PST(6,9); bar();
    if(w==0 && lane==0) publish(ws,bx,base+3);
    p3pre();
  } else if(ii<27){
    p3pre();
    const int lane=lane_id();
    const int m=xcd&1, cb=(ii<26)?8*(xcd>>1)+ii-18:32+(xcd>>1);
    const char* src=(m?wu:wg)+cb*256+(size_t)w*72*(F*2);
    float a[8]={0.f,0.f,0.f,0.f,0.f,0.f,0.f,0.f};
    u32x4 v[18];
    #pragma unroll
    for(int i=0;i<18;i++) gld(v[i],(lane>>4)*(F*2)+(lane&15)*16,src+(size_t)i*4*(F*2));
    wait_vm<0>();
    #pragma unroll
    for(int i=0;i<18;i++){ pin(v[i]);
      #pragma unroll
      for(int e=0;e<4;e++){ a[2*e]+=lo16(v[i][e]); a[2*e+1]+=hi16(v[i][e]); } }
    #pragma unroll
    for(int e=0;e<8;e++){ a[e]+=__shfl_xor(a[e],16); a[e]+=__shfl_xor(a[e],32); }
    LAS float* red=(LAS float*)(lds+65536);
    if(lane<16){
      #pragma unroll
      for(int e=0;e<8;e++) red[w*128+lane*8+e]=a[e]; }
    wait_lgkm0(); bar();
    if(w<2){ const int t_=w*64+lane; float s=0.f;
      #pragma unroll
      for(int i=0;i<8;i++) s+=red[i*128+t_];
      const unsigned hi=pk2(s,s)&0xffffu, lo=pk2(s-lo16(hi),0.f)&0xffffu;
      const int f=cb*128+t_; int j,t,l;
      if(f<4096){ j=f>>7; t=((f>>5)&3)*2+m; l=f&31; } else { j=(f-4096)>>4; t=8; l=(f&15)+16*m; }
      gstc1(hi|(lo<<16),(((j*3+(t>>2))*32+l)*4+(t&3))*4,ws+WS_CS); wait_vm<0>(); }
    bar();
    if(w==0 && lane==0) publish(ws,bx,base+3);
  } else {
    p3pre();
    const int lane=lane_id(), sk=xcd>>1;
    u32x4 v[16];
    #pragma unroll
    for(int i=0;i<16;i++){ const int o=((((xcd&1)*5+ii-27)*8+w)*16+i)*1024;
      gld(v[i],lane*16,wd+(o<1024*1152?(size_t)sk*1024*1152+o:(size_t)(4096+128*sk)*1152+(o-1024*1152))); }
    wait_vm<0>();
    #pragma unroll
    for(int i=0;i<16;i++) pin(v[i]);
  }
  STAMP(9); PST(6,10);
  const int sk=xcd>>1, t4=8*(xcd&1)+(ii&7), ng=ii>>3;
  unsigned vW4, pl4, tW4, tH4, oW4=0, oH4=0;
  auto mk4=[&](){ const int l=lane_id(); vW4=(unsigned)((8*(w&1)+(l>>3))*(D*2)+(((l&7)^(((l>>4)&1)<<2))*16));
    auto fs=[&](int i){ const int c=l-12+3*(ng&1)+(ng&2), c6=c>=6?c-6:c;
      return l<8?64*sk+8*((5*ng+l)&7)+i:(l<12||c6<4)?64*sk+16*(l<12?(l+ng)&3:c6)+8*(i>>1)+(l<12?4:6)+(i&1):240+8*sk+4*c6+i; };
    tW4=fs(w>>1)*(16*D*2); tH4=fs(2*(w>>2))*1024; };
  const char* w4a=wd+ng*256; const char* w4e=wd+1024; const char* h4=ws+WS_G+(size_t)(t4*4+(w&3))*288*1024;
  const unsigned l4w=lb+w*1024, l4h=lb+16384+((w&3)*4+2*(w>>2))*1024;
  auto ld4=[&](auto P,auto O,unsigned l16){ constexpr int p=decltype(P)::value, o=decltype(O)::value;
    if constexpr(o==0){ oH4=addlane<p>(tH4,l16); gldsx<slot4(p),0,true>(h4,oH4,l4h); }
    if constexpr(o==1) gldsx<slot4(p)+1024,1024,true>(h4,oH4,l4h);
    if constexpr(o==2){ oW4=addlane<p>(tW4,vW4); gldsx<slot4(p),0,false>(w4a,oW4,l4w); }
    if constexpr(o==3) gldsx<slot4(p)+8192,128,false>(w4a,oW4,l4w);
    if constexpr(o==4) gldsx<106496,0,false>(w4e,oW4,l4w); };
  auto ask4=[&](){ gld1c(pl4,(((lane_id()&7)*4+((t4>>1)&3))*8+xcd)*128,ws+WS_FLAG); };
  auto got4=[&](unsigned want){ pin(pl4); if(!__all(pl4-base-want<8u-want)) wait_progress(ws,((lane_id()&7)*4+((t4>>1)&3))*8+xcd,8,base,want); };
  {
    const int lane=lane_id();
    const int tt=4*(xcd&1)+(ii&3), j=8*(xcd>>1)+(ii>>2), T=tt*8+w;
    const unsigned voffS=(unsigned)((8*w+(lane>>3))*(F*2)+(((lane&7)^(((lane>>4)&1)<<2))*16));
    const unsigned xoff=lane*16; const char* xb=ws+WS_H+(size_t)T*36*1024;
    u32x4 x_[36], sp[5], cv[3];
    auto ldw=[&](int m,int kb,int ch,int ps){ glds16((m?uA:gA)+ch*128+(size_t)kb*64*F*2, voffS, lb+ps*16384+m*8192+w*1024); };
    if(w==0){
      int who; if(lane<18){ const int u=(lane>>1)*4+((2*tt+(lane&1))&3); who=(u%18)*8+(tt>>1)*2+u/18; } else who=((lane&2)?26:18+(j&7))*8+(xcd&6)+(lane&1);
      wait_progress(ws,who,22,base,3); }
    bar();
    STAMP(10); PST(6,11); PST(7,1);
    #pragma unroll
    for(int i=0;i<4;i++) gldc(x_[i],xoff,xb+i*1024);
    bar();
    #pragma unroll
    for(int i=4;i<8;i++) gldc(x_[i],xoff,xb+i*1024);
    auto lds_=[&](int i){ const unsigned l16=(lane_id()&31)*16;
      if(i<4) gldc(sp[i],l16+(lane_id()>>5)*(4*S*16),ws+WS_ST+(size_t)T*32*16+i*(S*16)); else if(i==4) gldc(sp[4],l16,ws+WS_ST+(size_t)T*32*16+8*S*16); else gldc(cv[i-5],l16,ws+WS_CS+j*1536+(i-5)*512); };
    if(w>=4) __builtin_amdgcn_s_setprio(1);
    const int q=(lane&15)>>2, p=lane&3, g2=(lane>>4)&1, hh=lane>>5;
    const unsigned a0_=(4*hh+q)*128+(((2*g2+(p>>1))^((q>>1)<<2))*16)+(p&1)*8, a0=lb+a0_, a1=lb+(a0_^64);
    const unsigned ac=lb+(4*hh+q)*64+(2*g2+(p>>1))*16+(p&1)*8;
    auto ldc=[&](int pi,int sl){ const unsigned l4=lane_id(); const int row0=pi*128+16*w-((pi==4&&w>=4)?64:0);
      const char* m=((l4&2)?wu:wg)+(size_t)(4096+16*j)*2+(size_t)row0*(F*2);
      glds16v(m+((l4>>2)*(F*2)+(l4&1)*16), lb+sl*8192+w*1024); };
    f32x16 acc[4]={};
    char* outb=ws+WS_G+(size_t)T*288*1024;
    constexpr int PD=4, NU=324;
    bf16x8 A[PD]; u32x4 eo, bm; float k1, iv, sm;
    auto unit=[](int u,int& kb,int& s,int& t){
      if(u<96){ kb=u>>4; s=(u>>2)&3; t=u&3; }
      else if(u<120){ int v=u-96; kb=6+(v>>3); s=(v>>1)&3; t=v&1; }
      else if(u<144){ int v=u-120; kb=6+(v>>3); s=(v>>1)&3; t=2+(v&1); }
      else if(u<216){ int v=u-144; kb=v>>3; s=(v>>1)&3; t=v&1; }
      else if(u<288){ int v=u-216; kb=v>>3; s=(v>>1)&3; t=2+(v&1); }
      else { int v=u-288; kb=v>>2; s=v&3; t=0; } };
    unsigned pb0=0,pb1=0;
    auto rd=[&](auto U){ constexpr int u=decltype(U)::value; int kb=0,s=0,t=0; unit(u,kb,s,t);
      if constexpr(u<288){ constexpr int kbc=(u<96)?(u>>4):(u<120)?6+((u-96)>>3):(u<144)?6+((u-120)>>3):(u<216)?((u-144)>>3):((u-216)>>3); constexpr int ps=(u<144)?kbc:(kbc+9)%10;
        if constexpr(u<96){ if constexpr((u&15)==0) pb0=addc<ps*16384>(a0); if constexpr((u&15)==2) pb1=addc<ps*16384>(a1); }
        else if constexpr((u&7)==0){ if constexpr(u<120||(u>=144&&u<216)) pb0=addc<ps*16384>(a0); else pb1=addc<ps*16384>(a1); }
        A[u%PD]=ldtr<1024>(((t&2)?pb1:pb0)+(t&1)*8192+s*2048); }
      else { constexpr int v=u-288, pi=v/8, sl=(pi<4)?16+pi:0;
        if constexpr((v&7)==0) pb0=addc<sl*8192>(ac);
        A[u%PD]=ldtr<512>(pb0+(v%8)*1024); } };
    float ea[2], eb[2], ep;
    auto job=[&](auto I,auto NE,const f32x16& g,const f32x16& u,int uo,int fs){ constexpr int i=decltype(I)::value, ne=decltype(NE)::value;
      constexpr int k=i/3, st=i%3, e=k-st;
      if constexpr(e>=0 && e<ne){
        if constexpr(st==0) ea[e&1]=__builtin_amdgcn_exp2f(g[e]*k1);
        if constexpr(st==1) eb[e&1]=__builtin_amdgcn_rcpf(fmaf(ea[e&1],iv,iv));
        if constexpr(st==2){ float v=opf(g[e]*u[uo+e]*eb[e&1]);
          if constexpr((e&1)==0) ep=v; else eo[(e>>1)&3]=pk2(ep,v);
          if constexpr((e&7)==7) gstc(eo,xoff,outb+(fs+(e>>3))*1024); } } };
    auto spread=[&](auto U,auto U0,auto L,const f32x16& g,const f32x16& uu,int fs){ constexpr int u=decltype(U)::value,u0=decltype(U0)::value,l=decltype(L)::value;
      if constexpr(u>=u0 && u<u0+l){ constexpr int b=(u-u0)*54/l, e=(u-u0+1)*54/l; static_for<b,e>([&](auto I){ job(I,IC(16),g,uu,0,fs); }); } };
    auto corr=[&](unsigned d,const f32x16& c){ u32x4 a={d,d&0xffffu,0u,0u}; return MFMA32(BF8(a),BF8(bm),c); };
    static_for<-PD,NU>([&](auto U){ constexpr int u=decltype(U)::value; constexpr int r=u+PD;
      if constexpr(r<=96 && (r&15)==0){ constexpr int kb=r>>4;
        constexpr int n=(kb==0)?4:(kb==1)?8:(kb==2)?12:(kb==3)?14:(kb==4)?11:(kb==5)?12:14;
        wait_vm4<n>(x_[4*kb],x_[4*kb+1],x_[4*kb+2],x_[4*kb+3]);
        bar(); }
      if constexpr(r==104){ wait_vm4<11>(x_[28],x_[29],x_[30],x_[31]); bar(); }
      if constexpr(r==112){ wait_vm4<6>(x_[32],x_[33],x_[34],x_[35]); bar(); }
      if constexpr(r==144){ wait_vm<3>(); bar(); STAMP(11); }
      if constexpr(r==144+40){ wait_vm<2>(); bar(); }
      if constexpr(r==288){ wait_vm<0>(); bar(); if(w==0 && lane==0) publish(ws,bx,base+5); }
      if constexpr(u==0) PST(7,2); if constexpr(u==32) PST(7,3); if constexpr(u==64) PST(7,4); if constexpr(u==96) PST(7,5); if constexpr(u==144) PST(7,6); if constexpr(u==180) PST(7,7); if constexpr(u==216) PST(7,8); if constexpr(u==252) PST(7,9); if constexpr(u==288) PST(7,10);
      if constexpr(u>=0){ int kb=0,s=0,t=0; unit(u,kb,s,t);
        if constexpr(u==144||u==145) acc[u-144]=corr(cv[1][u-144],f32x16{});
        if constexpr(u==216||u==217) acc[u-214]=corr(cv[1][2+u-216],f32x16{});
        if constexpr(u==288) acc[0]=corr(cv[2][0],f32x16{});
        if constexpr(u>=88&&u<92) acc[u-88]=corr(cv[0][u-88],acc[u-88]);
        acc[t]=MFMA32(A[u%PD],BF8(x_[u<288?4*kb+s:u-288]),acc[t]);
        SB(); }
      if constexpr(r<NU) rd(IC(r));
      if constexpr(r<96){ if constexpr((r&1)==0){ constexpr int kb=r>>4, sl=(r&15)>>1;
          constexpr int op[6][8]={{8,9,10,11,-1,-2,12,13},{14,15,-1,-2,40,41,42,43},{44,16,17,18,19,-1,-2,45},{20,21,22,23,-1,-2,24,25},{26,27,-1,-2,28,29,30,31},{-1,-2,32,33,34,35,46,47}}, o=op[kb][sl];
          if constexpr(o<0) ldw(-1-o,kb+3,0,kb+3); else if constexpr(o<40) gldc(x_[o],xoff,xb+o*1024); else lds_(o-40); } }
      else if constexpr(r<128){ constexpr int o=(r-96)&7; constexpr int slot=(o==1)?0:(o==3)?1:(o==6)?2:-1;
        if constexpr(slot>=0){ constexpr int n=((r-96)>>3)*3+slot, i=n>>1; ldw(n&1,i,1,i==0?9:i-1); } }
      if constexpr(r==130) ldw(0,6,1,5);
      if constexpr(r==133) ldw(1,6,1,5);
      if constexpr(r==80) pin(cv[0]);
      if constexpr(r==112){ pin(cv[1]); pin(cv[2]); }
      if constexpr(r>=64 && r<76){
        if constexpr(r==64){ pin(sp[0]); pin(sp[1]); pin(sp[2]); pin(sp[3]); pin(sp[4]); }
        if constexpr(r==65){ float a=__uint_as_float(hh?sp[4][2]:sp[4][0]);
          #pragma unroll
          for(int i=0;i<4;i++) a+=__uint_as_float(sp[i][0])+__uint_as_float(sp[i][2]);
          sm=xsum32(a)*(1.f/18); }
        if constexpr(r==69){ float d=__uint_as_float(hh?sp[4][2]:sp[4][0])-sm; float a=__uint_as_float(hh?sp[4][3]:sp[4][1]), b=d*d;
          #pragma unroll
          for(int i=0;i<8;i++){ d=__uint_as_float(sp[i>>1][(i&1)*2])-sm; a+=__uint_as_float(sp[i>>1][(i&1)*2+1]); b=fmaf(d,d,b); }
          a=fmaf(b,32.f,a);
          iv=xsum32(a)*(1.f/D)+1e-5f; }
        if constexpr(r==74){ k1=__builtin_amdgcn_rsqf(iv)*-1.44269504f;
          const unsigned mh=pk2(-sm,-sm); const float ml=-sm-lo16(mh);
          bm=u32x4{hh?0u:mh, hh?0u:(pk2(ml,ml)&0xffffu), 0u, 0u}; } }
      if constexpr(r==144+1) ldw(0,7,1,6);
      if constexpr(r==144+4) ldw(1,7,1,6);
      if constexpr(r==144+7) ldw(0,8,1,7);
      if constexpr(r==144+10) ldw(1,8,1,7);
      if constexpr(r==144+42) ldc(0,16);
      if constexpr(r==144+46) ldc(1,17);
      if constexpr(u==216+15){ SB(); wait_vm<2>(); bar(); if(w==0 && lane==0) publish(ws,bx,base+4); }
      if constexpr(u==244){ const int e=(ii*8+w)*41+lane_id(), e2=(lane_id()<41&&e<10368)?e:0, row=e2/9;
        glds4(wd,(row<1024?1024*sk+row:3072+128*sk+row)*(D*2)+(e2-row*9)*128,lb+8192+w*256); }
      if constexpr(u==270) ask4();
      if constexpr(u==216+16) ldc(2,18);
      if constexpr(u==216+19) ldc(3,19);
      if constexpr(u==216+22) ldc(4,0);
      if constexpr(u==289) mk4();
      if constexpr(u==290) got4(4);
      if constexpr(u>=290&&u<316&&(u&1)==0){ constexpr int n=(u-290)>>1, p=n<5?0:(n-1)>>2, o=n<5?n:(n-1)&3; ld4(IC(p),IC(o),xoff); }
      spread(U,IC(121),IC(23),acc[0],acc[1],8*j);
      spread(U,IC(146),IC(34),acc[2],acc[3],8*j+2);
      spread(U,IC(218),IC(66),acc[0],acc[1],8*j+4);
      spread(U,IC(290),IC(34),acc[2],acc[3],8*j+6);
      SB();
    });
    PST(7,11);
    static_for<0,30>([&](auto I){ job(I,IC(8),acc[0],acc[0],8,256+j); });
    PST(7,12);
  }
  STAMP(12); PST(7,13); PST(8,1);
  {
    const int lane=lane_id(); const unsigned l16=lane*16;
    const int tg=w&3, ch=w>>2;
    const int q=(lane&15)>>2, p=lane&3, g2=(lane>>4)&1, hh=lane>>5;
    const unsigned a0_=(4*hh+q)*128+(((2*g2+(p>>1))^((q>>1)<<2))*16)+(p&1)*8, a0=lb+ch*8192+a0_, a1=lb+ch*8192+(a0_^64), aE=lb+106496+(a0_^(ch*64)), aH=lb+16384+tg*4096+l16;
    f32x16 c[3]={};
    constexpr int PD=4, NU=164;
    bf16x8 A[PD], B[4]; unsigned pb0=0,pb1=0,pbH=0;
    const unsigned keep=ng<2?~0u:0u;
    static_for<-PD,NU>([&](auto U){ constexpr int u=decltype(U)::value, r=u+PD;
      constexpr int pr=stg(r), orr=r-st0(pr), nr=(pr&3)?2:3;
      if constexpr(r<NU && orr==0){
        constexpr int n=(pr<16?4+((pr+2)%4==0):0)+(pr<17?4+((pr+1)%4==0):0);
        wait_vm<n>(); bar();
        if constexpr(pr==0){ STAMP(13); PST(7,14); PST(8,2); }
        if constexpr(pr==2){ if(w==0 && lane==0) publish(ws,bx,base+6); }
        if constexpr(pr==5) got4(5);
        if constexpr(pr==9) got4(6);
        pb0=addc<slot4(pr)>(a0); pb1=addc<slot4(pr)>(a1); pbH=addc<slot4(pr)>(aH); }
      if constexpr(u==0) PST(8,3); if constexpr(u==36) PST(8,4); if constexpr(u==72) PST(8,5); if constexpr(u==108) PST(8,6); if constexpr(u==144) PST(8,7);
      if constexpr(u>=0){ constexpr int pu=stg(u), ou=u-st0(pu), nu=(pu&3)?2:3, s=ou/nu, t=ou%nu;
        if constexpr(pu==16&&t==2){ u32x4 b=__builtin_bit_cast(u32x4,B[s]); b&=keep; c[2]=MFMA32(A[u%PD],BF8(b),c[2]); }
        else c[t]=MFMA32(A[u%PD],B[s],c[t]);
        SB(); }
      if constexpr(r<NU){ constexpr int s=orr/nr, t=orr%nr;
        if constexpr(t==0) B[s]=*(LAS bf16x8*)(size_t)(pbH+s*1024);
        A[r%PD]=ldtr<1024>((t==0?pb0:t==1?pb1:aE)+s*2048); }
      if constexpr(r<NU && pr+3<18){
        if constexpr((pr==3||pr==7)&&orr==0) ask4();
        if constexpr(orr==1||orr==3) ld4(IC(pr+3),IC(orr>>1),l16);
        if constexpr(orr==5||orr==6||(orr==7&&(pr+3)%4==0)) ld4(IC(pr+3),IC(orr-3),l16); }
      SB();
    });
    __builtin_amdgcn_s_setprio(0);
    STAMP(14); PST(8,8);
    char* xb=ws+WS_X+(size_t)((t4*4+ng)*4+tg)*4*8192+ch*4096;
    char* rb=ws+WS_R+(size_t)t4*256*1024;
    u32x4 rr[4];
    { char* d=rb+((tg*4+ch*2)*16+sk*4+ng)*1024; gstc(__builtin_bit_cast(u32x4,pack8(c[2],0)),l16,d); gstc(__builtin_bit_cast(u32x4,pack8(c[2],8)),l16,d+16384); }
    if(tg!=sk){ char* d=xb+sk*8192;
      gstc(__builtin_bit_cast(u32x4,pack8(c[0],0)),l16,d); gstc(__builtin_bit_cast(u32x4,pack8(c[0],8)),l16,d+1024);
      gstc(__builtin_bit_cast(u32x4,pack8(c[1],0)),l16,d+2048); gstc(__builtin_bit_cast(u32x4,pack8(c[1],8)),l16,d+3072);
    } else { const char* hr=ws+WS_H+((size_t)(t4*4+tg)*36+ng*8+ch*4)*1024;
      #pragma unroll
      for(int i=0;i<4;i++) gldc(rr[i],l16,hr+i*1024); }
    PST(8,10);
    wait_vm<0>(); PST(8,11); bar();
    if(w==0 && lane==0) publish(ws,bx,base+7);
    if(tg==sk){
      wait_progress(ws,ii*8+(lane&3)*2+(xcd&1),4,base,7);
      STAMP(15); PST(8,12);
      u32x4 pv[3][4];
      #pragma unroll
      for(int s=0;s<3;s++){ const char* d=xb+(s+(s>=sk?1:0))*8192;
        #pragma unroll
        for(int i=0;i<4;i++) gldc(pv[s][i],l16,d+i*1024); }
      wait_vm<0>();
      #pragma unroll
      for(int i=0;i<4;i++){ pin(rr[i]); pin(pv[0][i]); pin(pv[1][i]); pin(pv[2][i]); }
      PST(8,13);
      char* orow=outp+((size_t)((t4*4+tg)*32+(lane&31))*D+ng*128+ch*64+8*hh)*2;
      #pragma unroll
      for(int fi=0;fi<4;fi++){ const f32x16& a=c[fi>>1]; const int o=8*(fi&1);
        float v[8];
        #pragma unroll
        for(int e=0;e<4;e++){
          v[2*e]=a[o+2*e]+lo16(rr[fi][e])+lo16(pv[0][fi][e])+lo16(pv[1][fi][e])+lo16(pv[2][fi][e]);
          v[2*e+1]=a[o+2*e+1]+hi16(rr[fi][e])+hi16(pv[0][fi][e])+hi16(pv[1][fi][e])+hi16(pv[2][fi][e]); }
        const auto s0=__builtin_amdgcn_permlane32_swap(pk2(v[0],v[1]),pk2(v[4],v[5]),false,false), s1=__builtin_amdgcn_permlane32_swap(pk2(v[2],v[3]),pk2(v[6],v[7]),false,false);
        *(GAS u32x4*)(orow+fi*32)=u32x4{s0[0],s1[0],s0[1],s1[1]}; }
      PST(8,14);
    } else if(w==((sk+1)&3)){
      u32x2 hr[2];
      const int n=(lane&15)+16*hh, hf=(lane>>4)&1;
      { const char* hs=ws+WS_H+((size_t)(t4*4+sk)*36+32+ng)*1024; gld2c(hr[0],n*16+hf*8,hs); gld2c(hr[1],n*16+hf*8,hs+512); }
      wait_progress(ws,((lane>>2&3)*8+(ii&7))*8+(lane&3)*2+(xcd&1),16,base,7);
      u32x4 pv[8][2];
      { const char* d=rb+(sk*4+ng)*16384;
        #pragma unroll
        for(int i=0;i<8;i++){ gldc(pv[i][0],(lane&31)*16+hh*1024,d+i*2048); gldc(pv[i][1],(lane&31)*16+hh*1024,d+i*2048+512); } }
      u32x4 sel; { const unsigned o=(((lane>>3)^(lane>>4))&1)?0u:((lane&1)?0x3f800000u:0x00003f80u); const int j=(lane&7)>>1; sel=u32x4{j==0?o:0u,j==1?o:0u,j==2?o:0u,j==3?o:0u}; }
      f32x4 e0={},e1={};
      static_for<0,8>([&](auto I){ constexpr int i=decltype(I)::value;
        wait_vm2<14-2*i>(pv[i][0],pv[i][1]);
        e0=MFMA16(BF8(sel),BF8(pv[i][0]),e0); e1=MFMA16(BF8(sel),BF8(pv[i][1]),e1); });
      pin(hr[0]); pin(hr[1]);
      *(GAS u32x4*)(outp+((size_t)((t4*4+sk)*32+n)*D+512+ng*16+hf*8)*2)=
        u32x4{pk2(e0[0]+lo16(hr[0][0]),e0[1]+hi16(hr[0][0])),pk2(e0[2]+lo16(hr[0][1]),e0[3]+hi16(hr[0][1])),pk2(e1[0]+lo16(hr[1][0]),e1[1]+hi16(hr[1][0])),pk2(e1[2]+lo16(hr[1][1]),e1[3]+hi16(hr[1][1]))};
    }
  }
  STAMP_OUT(0);
}

extern "C" void kernel_launch(void* const* d_in, const int* in_sizes, int n_in, void* d_out, int out_size, void* d_ws, size_t ws_size, hipStream_t stream){
  (void)hipFuncSetAttribute((const void*)mega,hipFuncAttributeMaxDynamicSharedMemorySize,163840);
  mega<<<256,512,163840,stream>>>((const char*)d_in[0],(const char*)d_in[1],(const char*)d_in[2],(const char*)d_in[3],(const char*)d_in[5],(const char*)d_in[6],(const char*)d_in[7],(char*)d_ws,(char*)d_out);
}
```

```cpp
#include <hip/hip_runtime.h>
#include <hip/hip_bf16.h>
#include <type_traits>
typedef unsigned short u16;
typedef __attribute__((ext_vector_type(4))) short s16x4;
typedef __attribute__((ext_vector_type(8))) __bf16 bf16x8;
typedef __attribute__((ext_vector_type(2))) __bf16 bf16x2;
typedef __attribute__((ext_vector_type(2))) float f32x2;
typedef __attribute__((ext_vector_type(2))) unsigned u32x2;
typedef __attribute__((ext_vector_type(4))) unsigned u32x4;
typedef __attribute__((ext_vector_type(4))) float f32x4;
typedef __attribute__((ext_vector_type(16))) float f32x16;
#define LAS __attribute__((address_space(3)))
#define GAS __attribute__((address_space(1)))
#define DI __device__ __forceinline__
#define RLX_AGENT __ATOMIC_RELAXED, __HIP_MEMORY_SCOPE_AGENT

constexpr unsigned CODE_PF=73728;
constexpr int S=2048, D=576, F=4608;

template<int I,int N,class Fn> DI void static_for(Fn&& f){ if constexpr(I<N){ f(std::integral_constant<int,I>{}); static_for<I+1,N>(f);} }
#define IC(n) std::integral_constant<int,(n)>{}
#define SB() __builtin_amdgcn_sched_barrier(0)
#define MFMA32(a,b,c) __builtin_amdgcn_mfma_f32_32x32x16_bf16((a),(b),(c),0,0,0)
#define MFMA16(a,b,c) __builtin_amdgcn_mfma_f32_16x16x32_bf16((a),(b),(c),0,0,0)
#define BF8(v) __builtin_bit_cast(bf16x8,(v))
DI unsigned pk2(float a,float b){ f32x2 f={a,b}; return __builtin_bit_cast(unsigned,__builtin_convertvector(f,bf16x2)); }
DI float lo16(unsigned u){ return __uint_as_float(u<<16); }
DI float hi16(unsigned u){ return __uint_as_float(u&0xffff0000u); }
DI float opf(float v){ asm("":"+v"(v)); return v; }
DI int lane_id(){ int l; asm volatile("v_mbcnt_lo_u32_b32 %0, -1, 0\n\tv_mbcnt_hi_u32_b32 %0, -1, %0":"=v"(l)); return l; }
template<int N> DI void wait_vm(){ asm volatile("s_waitcnt vmcnt(%0)"::"n"(N):"memory"); }
DI void wait_lgkm0(){ asm volatile("s_waitcnt lgkmcnt(0)":::"memory"); }
DI void bar(){ __builtin_amdgcn_s_barrier(); }
DI s16x4 trd(LAS char* p){ return __builtin_amdgcn_ds_read_tr16_b64_v4i16((LAS s16x4*)p); }
DI bf16x8 cat(s16x4 a,s16x4 b){ return BF8(__builtin_shufflevector(a,b,0,1,2,3,4,5,6,7)); }
template<int TS> DI bf16x8 ldtr(unsigned p){ return cat(trd((LAS char*)(size_t)p),trd((LAS char*)(size_t)p+TS)); }
template<int C> DI unsigned addc(unsigned v){ unsigned r; asm volatile("v_add_u32 %0, %1, %2":"=v"(r):"n"(C),"v"(v)); return r; }
DI void glds16(const char* sb, unsigned voff, unsigned ldsaddr){
  asm volatile("s_mov_b32 m0, %2\n\ts_nop 0\n\tglobal_load_lds_dwordx4 %0, %1"::"v"(voff),"s"(sb),"s"(ldsaddr):"memory"); }
template<int C,int OFF,bool SC> DI void gldsx(const char* sb, unsigned voff, unsigned lw){
  if constexpr(SC) asm volatile("s_add_i32 m0, %2, %3\n\ts_nop 0\n\tglobal_load_lds_dwordx4 %0, %1 offset:%4 sc1"::"v"(voff),"s"(sb),"s"(lw),"n"(C-OFF),"n"(OFF):"memory","scc");
  else asm volatile("s_add_i32 m0, %2, %3\n\ts_nop 0\n\tglobal_load_lds_dwordx4 %0, %1 offset:%4"::"v"(voff),"s"(sb),"s"(lw),"n"(C-OFF),"n"(OFF):"memory","scc"); }
template<int L> DI unsigned addlane(unsigned tab,unsigned v){ unsigned r,t; asm volatile("v_readlane_b32 %1, %2, %4\n\ts_nop 1\n\tv_add_u32 %0, %1, %3":"=v"(r),"=&s"(t):"v"(tab),"v"(v),"n"(L)); return r; }
DI void glds4(const char* sb, unsigned voff, unsigned ldsaddr){
  asm volatile("s_mov_b32 m0, %2\n\ts_nop 0\n\tglobal_load_lds_dword %0, %1"::"v"(voff),"s"(sb),"s"(ldsaddr):"memory"); }
DI void glds16q(const char* sb, unsigned voff, unsigned ldsaddr){
  asm volatile("s_mov_b32 m0, %2\n\ts_mov_b64 exec, 0xffff\n\tglobal_load_lds_dwordx4 %0, %1\n\ts_mov_b64 exec, -1"::"v"(voff),"s"(sb),"s"(ldsaddr):"memory"); }
DI void glds16v(const char* vp, unsigned ldsaddr){
  asm volatile("s_mov_b32 m0, %1\n\ts_nop 0\n\tglobal_load_lds_dwordx4 %0, off"::"v"(vp),"s"(ldsaddr):"memory"); }
DI void gld(u32x4& a,unsigned voff,const char* sb){ asm volatile("global_load_dwordx4 %0, %1, %2":"=&v"(a):"v"(voff),"s"(sb):"memory"); }
DI void gldc(u32x4& a,unsigned voff,const char* sb){ asm volatile("global_load_dwordx4 %0, %1, %2 sc1":"=&v"(a):"v"(voff),"s"(sb):"memory"); }
DI void gstc(const u32x4& a,unsigned voff,char* sb){ asm volatile("global_store_dwordx4 %1, %0, %2 sc1\n\ts_nop 1"::"v"(a),"v"(voff),"s"(sb):"memory"); }
DI void gsto(const u32x4& a,unsigned voff,char* sb){ asm volatile("global_store_dwordx4 %1, %0, %2\n\ts_nop 1"::"v"(a),"v"(voff),"s"(sb):"memory"); }
DI void gstc2(const u32x2& a,unsigned voff,char* sb){ asm volatile("global_store_dwordx2 %1, %0, %2 sc1"::"v"(a),"v"(voff),"s"(sb):"memory"); }
DI void gstc1(unsigned a,unsigned voff,char* sb){ asm volatile("global_store_dword %1, %0, %2 sc1"::"v"(a),"v"(voff),"s"(sb):"memory"); }
DI void gld1(unsigned& a,unsigned voff,const char* sb){ asm volatile("global_load_dword %0, %1, %2":"=&v"(a):"v"(voff),"s"(sb):"memory"); }
DI void gld1c(unsigned& a,unsigned voff,const char* sb){ asm volatile("global_load_dword %0, %1, %2 sc1":"=&v"(a):"v"(voff),"s"(sb):"memory"); }
DI void gld2c(u32x2& a,unsigned voff,const char* sb){ asm volatile("global_load_dwordx2 %0, %1, %2 sc1":"=&v"(a):"v"(voff),"s"(sb):"memory"); }
template<int ST> DI void gld4m(unsigned* a,unsigned voff,const char* sb,unsigned m){
  asm volatile("s_cmp_eq_u32 %6, 0\n\ts_cbranch_scc1 .Lskip%=\n\tglobal_load_dword %0, %4, %5 sc1\n\tglobal_load_dword %1, %4, %5 offset:%7 sc1\n\tglobal_load_dword %2, %4, %5 offset:%8 sc1\n\tglobal_load_dword %3, %4, %5 offset:%9 sc1\n.Lskip%=:"
    :"=&v"(a[0]),"=&v"(a[1]),"=&v"(a[2]),"=&v"(a[3]):"v"(voff),"s"(sb),"s"(m),"n"(ST),"n"(2*ST),"n"(3*ST):"memory","scc"); }
template<int N> DI void wait_vm4m(unsigned* a,unsigned m){
  asm volatile("s_cmp_eq_u32 %4, 0\n\ts_cbranch_scc1 .Lskip%=\n\ts_waitcnt vmcnt(%5)\n.Lskip%=:":"+v"(a[0]),"+v"(a[1]),"+v"(a[2]),"+v"(a[3]):"s"(m),"n"(N):"memory","scc"); }
DI void gst4m(const u32x2& a,const u32x2& b,const u32x2& c,const u32x2& d,unsigned voff,char* sb,unsigned m){
  asm volatile("s_cmp_eq_u32 %6, 0\n\ts_cbranch_scc1 .Lskip%=\n\tglobal_store_dwordx2 %4, %0, %5 sc1\n\tglobal_store_dwordx2 %4, %1, %5 offset:512 sc1\n\tglobal_store_dwordx2 %4, %2, %5 offset:1024 sc1\n\tglobal_store_dwordx2 %4, %3, %5 offset:1536 sc1\n.Lskip%=:"
    ::"v"(a),"v"(b),"v"(c),"v"(d),"v"(voff),"s"(sb),"s"(m):"memory","scc"); }
template<class T> DI void pin(T& a){ asm volatile("":"+v"(a)); }
template<int N> DI void wait_vm4(u32x4& a,u32x4& b,u32x4& c,u32x4& d){ asm volatile("s_waitcnt vmcnt(%4)":"+v"(a),"+v"(b),"+v"(c),"+v"(d):"n"(N):"memory"); }
template<int N> DI void wait_vm3(u32x4& a,u32x4& b,u32x4& c){ asm volatile("s_waitcnt vmcnt(%3)":"+v"(a),"+v"(b),"+v"(c):"n"(N):"memory"); }
template<int N> DI void wait_vm2(u32x4& a,u32x4& b){ asm volatile("s_waitcnt vmcnt(%2)":"+v"(a),"+v"(b):"n"(N):"memory"); }
template<int C> DI float dppf(float v){ return __uint_as_float(__builtin_amdgcn_update_dpp(0u,__float_as_uint(v),C,0xf,0xf,true)); }
DI float sum8(float v){ v+=dppf<0xB1>(v); v+=dppf<0x4E>(v); v+=dppf<0x141>(v); return v; }
DI float xsum32(float v){ const auto r=__builtin_amdgcn_permlane32_swap(__float_as_uint(v),__float_as_uint(v),false,false); return __uint_as_float(r[0])+__uint_as_float(r[1]); }
DI bf16x8 pack8(const f32x16& c,int o){ u32x4 r={pk2(c[o],c[o+1]),pk2(c[o+2],c[o+3]),pk2(c[o+4],c[o+5]),pk2(c[o+6],c[o+7])}; return BF8(r); }

constexpr size_t WS_FLAG=0;
constexpr size_t WS_A=32768, WS_Q=36864;
constexpr int    KVP=5632;
constexpr size_t WS_KVP=65536;
constexpr size_t WS_O=WS_KVP+256*KVP;
constexpr size_t WS_H=WS_O+(size_t)S*D*2;
constexpr size_t WS_ST=WS_H+(size_t)S*D*2;
constexpr size_t WS_CS=WS_ST+9*S*16;
constexpr size_t WS_X=WS_CS+32*3*32*16;
constexpr size_t WS_R=WS_X+64*16*8192;
constexpr size_t WS_G=WS_R+16*256*1024;

DI void wait_progress(char* ws,int who,int n,unsigned base,unsigned want,bool serve=false){
  const int lane=lane_id();
  const GAS unsigned* p=(const GAS unsigned*)(ws+WS_FLAG)+(lane<n?who:0)*32;
  for(unsigned spins=0;spins<(1u<<18);spins++){
    const unsigned v=__hip_atomic_load(p,RLX_AGENT);
    if(serve){ unsigned qv[4]; unsigned long long av[4];
      #pragma unroll
      for(int i=0;i<4;i++){ qv[i]=__hip_atomic_load((const GAS unsigned*)(ws+WS_Q)+i*64+lane,RLX_AGENT); av[i]=__hip_atomic_load((const GAS unsigned long long*)(ws+WS_A)+i*64+lane,RLX_AGENT); }
      #pragma unroll
      for(int i=0;i<4;i++){ const unsigned long long a=((unsigned long long)base<<32)|qv[i]; if(av[i]!=a) __hip_atomic_store((GAS unsigned long long*)(ws+WS_A)+i*64+lane,a,RLX_AGENT); } }
    if(__all(lane>=n || v-base-want<8u-want)) break;
    __builtin_amdgcn_s_sleep(1);
  }
}
DI void meet4(LAS unsigned* c){
  if(lane_id()==0) __hip_atomic_fetch_add(c,1u,__ATOMIC_RELAXED,__HIP_MEMORY_SCOPE_WORKGROUP);
  while(__builtin_amdgcn_readfirstlane(__hip_atomic_load(c,__ATOMIC_RELAXED,__HIP_MEMORY_SCOPE_WORKGROUP))<4u) __builtin_amdgcn_s_sleep(1);
  asm volatile("":::"memory"); }
DI void publish(char* ws,int bx,unsigned v){ __hip_atomic_store((GAS unsigned*)(ws+WS_FLAG)+bx*32,v,RLX_AGENT); }

constexpr int slot4(int p){ return (p&3)==3?114688:8192+(p&3)*32768; }
constexpr int ex4(int p){ return (p<=12&&(p&3)==0)||p==13; }
constexpr int st0(int p){ return 8*p+4*(p>13?5:(p+3)>>2); }
constexpr int stg(int u){ int p=0; while(p<17&&st0(p+1)<=u) p++; return p; }

__global__ __launch_bounds__(512,2) void mega(const char* __restrict__ x,const char* __restrict__ wqkv,const char* __restrict__ wphi,const char* __restrict__ wo,
                                              const char* __restrict__ wg,const char* __restrict__ wu,const char* __restrict__ wd,char* __restrict__ ws,char* __restrict__ outp){
  extern __shared__ __attribute__((aligned(16))) char lds_[];
  LAS char* lds=(LAS char*)lds_; const unsigned lb=(unsigned)(size_t)lds;
  const int w=__builtin_amdgcn_readfirstlane(threadIdx.x>>6);
  const int bx=blockIdx.x, xcd=bx&7, ii=bx>>3;
  const char* pc0; asm volatile("s_getpc_b64 %0":"=s"(pc0));
  unsigned base;
  {
    const int lane=lane_id(), hh=lane>>5, r=lane&31; const unsigned l16=lane*16;
    const int hg=4*(xcd&1)+(ii&3), tk=8*(xcd>>1)+(ii>>2), tg=w&1, hp=(w>>1)&1;
    constexpr int RB=73728, SL=5120, TI=114688, KVI=156672, RS=162832, QC=163824, BW=163832, PD=2;
    const int q=(lane&15)>>2, p=lane&3, g2=(lane>>4)&1, sg=2*g2+(p>>1), kw=w&3;
    const int rot=((ii>>2)+2*(ii&3))%9;
    auto ub=[&](int u){ const int v=u+rot; return wqkv+hg*144+(size_t)(v>=9?v-9:v)*(64*3456); };
    const unsigned ab=lb+RB+(8*hh+q)*320+72*hp+32*(p&1), aB_=lb+tg*36864+hh*512;
    const int tl=tg*32+r, sig=(((tl>>1)&1)<<2)|(tl&1)|(((tl>>2)&1)<<1);
    LAS char* trow=lds+TI+((2*hp+hh)*64+tl)*128;
    unsigned vk[5];
    auto mkvk=[&](){
      #pragma unroll
      for(int i=0;i<5;i++){ const unsigned c=i*64+lane, row=(c*205u)>>12, cc=c-row*20, t=cc>=10, c2=cc-10*t; vk[i]=(kw*16+row)*3456+1152*(1+t)+(c2<8?c2:8)*16; } };
    auto dma=[&](int u){
      #pragma unroll
      for(int i=0;i<5;i++) glds16(ub(u),vk[i],lb+RB+(4*(u&3)+kw)*SL+i*1024); };
    u32x4 pw[6];
    constexpr int PHS=1440;
    auto ldpw=[&](){ const unsigned b1=lb+KVI+kw*PHS+(hh*16+q)*40+32, b0=b1-32+8*sg;
      const unsigned mp=(((r>>2)&1)==hh)?~0u:0u, m0=hh?0u:mp, m1=hh?mp:0u;
      #pragma unroll
      for(int n=0;n<6;n++){ const unsigned b=(n<3?b0:b1)+(n%3)*320;
        const u32x2 lo=__builtin_bit_cast(u32x2,trd((LAS char*)(size_t)b));
        if(n%3<2){ const u32x2 hi=__builtin_bit_cast(u32x2,trd((LAS char*)(size_t)(b+160))); pw[n]=u32x4{lo[0]&(n%3?mp:m0),lo[1]&mp,hi[0]&mp,hi[1]&mp}; }
        else pw[n]=u32x4{lo[0]&mp,lo[1]&m1,0u,0u}; } };
    auto feat=[&](const f32x16& c0,const f32x16& c1,float rs,float* e1,float* e2,float& s1,float& s2){
      f32x16 p0={},p1={};
      { const bf16x8 b0=pack8(c0,0), b1=pack8(c0,8), b2=pack8(c1,0);
        p0=MFMA32(BF8(pw[0]),b0,p0); p1=MFMA32(BF8(pw[3]),b0,p1);
        p0=MFMA32(BF8(pw[1]),b1,p0); p1=MFMA32(BF8(pw[4]),b1,p1);
        p0=MFMA32(BF8(pw[2]),b2,p0); p1=MFMA32(BF8(pw[5]),b2,p1); }
      float pv[18];
      #pragma unroll
      for(int i=0;i<16;i++) pv[i]=p0[i];
      pv[16]=p1[0]; pv[17]=p1[1];
      float mx=pv[0],mn=pv[0];
      #pragma unroll
      for(int i=1;i<18;i++){ mx=fmaxf(mx,pv[i]); mn=fminf(mn,pv[i]); }
      const float L=1.44269504f*rs, a=-mx*L, b=mn*L; s1=0.f; s2=0.f;
      #pragma unroll
      for(int i=0;i<18;i++){ e1[i]=__builtin_amdgcn_exp2f(fmaf(pv[i],L,a)); e2[i]=__builtin_amdgcn_exp2f(fmaf(pv[i],-L,b)); s1+=e1[i]; s2+=e2[i]; } };
    char* gd=ws+WS_KVP+(size_t)(hg*32+tk)*KVP+kw*1408; char* dst=ws+WS_O+(size_t)(tk*2+tg)*36*1024; const int pr=2*hg+hp;
    u32x2 av; u32x4 xv[18]; unsigned cp;
    if(w<4){
      __builtin_amdgcn_s_setprio(3);
      gld2c(av,0u,ws+WS_A+bx*8);
      const char* xs=x+(size_t)(tk*64+w*16)*1152; const unsigned xo=(lane>>3)*1152+(lane&7)*16;
      #pragma unroll
      for(int i=0;i<18;i++){ const int c=i%9+rot; gld(xv[i],xo,xs+(c>=9?c-9:c)*128+(i/9)*9216); }
      __builtin_amdgcn_s_setprio(0);
    } else {
      { const unsigned o=(ii*4+kw)*8192+lane*128; gld1(cp,o<CODE_PF?o:0u,pc0); }
      mkvk(); dma(0); dma(1);
    }
    int w2=w; asm volatile("":"+s"(w2));
    unsigned aB[4];
    #pragma unroll
    for(int i=0;i<4;i++) aB[i]=aB_+(r^(2*i+hh))*16;
    if(w2<4){
      const unsigned mk=__builtin_amdgcn_readfirstlane((bx==0&&w==0)?~0u:0u);
      unsigned pg[4], qv[4], nb=0;
      gld4m<128>(pg,lane*512,ws+WS_FLAG,mk);
      mkvk();
      unsigned vq[3];
      { unsigned c=w*64+lane, row=c/9; vq[0]=row*3456+(c-row*9)*16; c+=256; row=c/9; vq[1]=row*3456+(c-row*9)*16; c=512+16*w+(lane&15); row=c/9; vq[2]=row*3456+(c-row*9)*16; }
      auto ldq=[&](int kg,int gp){ const char* s=ub(kg); const unsigned L=lb+RB+gp*9216;
        glds16(s,vq[0],L+w*1024); glds16(s,vq[1],L+4096+w*1024); glds16q(s,vq[2],L+8192+w*256); };
      wait_vm<18>(); pin(av);
      const unsigned req=av[0]+1u;
      if(w==0&&lane==0) gstc1(req,0u,ws+WS_Q+bx*4);
      auto var=[&](int h){ f32x4 ga={},gb={};
        #pragma unroll
        for(int i=0;i<9;i++){ const s16x4 lo=__builtin_bit_cast(s16x4,(u32x2{xv[9*h+i][0],xv[9*h+i][1]})), hi=__builtin_bit_cast(s16x4,(u32x2{xv[9*h+i][2],xv[9*h+i][3]}));
          ga=__builtin_amdgcn_mfma_f32_4x4x4bf16_1k(lo,lo,ga,0,0,0); gb=__builtin_amdgcn_mfma_f32_4x4x4bf16_1k(hi,hi,gb,0,0,0); }
        ga+=gb;
        const float qq=sum8((lane&2)?((lane&1)?ga[3]:ga[2]):((lane&1)?ga[1]:ga[0]));
        if((lane&7)==0) *(LAS float*)(lds+RS+(w*16+8*h+(lane>>3))*4)=__builtin_amdgcn_rsqf(qq*(1.f/D)+1e-5f); };
      { const bool dg=(lane>>4)==((lane&15)>>2); const unsigned o=dg?((lane&1)?0x3f800000u:0x00003f80u):0u, o0=(lane&2)?0u:o, o1=(lane&2)?o:0u, on=dg?0x3f803f80u:0u;
        const bf16x8 sa=BF8((u32x4{o0,o1,0u,0u})), sb=BF8((u32x4{0u,0u,o0,o1})), sq=BF8((u32x4{on,on,on,on}));
        static_for<0,2>([&](auto H){ constexpr int h=decltype(H)::value;
          f32x4 sm={};
          static_for<0,3>([&](auto G){ constexpr int g=9*h+3*decltype(G)::value; wait_vm3<15-g>(xv[g],xv[g+1],xv[g+2]);
            sm=MFMA16(sq,BF8(xv[g]),sm); sm=MFMA16(sq,BF8(xv[g+1]),sm); sm=MFMA16(sq,BF8(xv[g+2]),sm); SB(); });
          const float m=sum8(sm[0])*(-1.f/D); const f32x4 nm={m,m,m,m};
          const int t5=(w&1)*16+8*h+(lane>>3), sub=lane&7;
          LAS char* xd=lds+((w>>1)*36+(sub>>1))*1024+((sub&1)*32+(t5^sub))*16;
          #pragma unroll
          for(int i=0;i<9;i++){ const f32x4 a=MFMA16(sa,BF8(xv[9*h+i]),nm), b=MFMA16(sb,BF8(xv[9*h+i]),nm);
            xv[9*h+i]=u32x4{pk2(a[0],a[1]),pk2(a[2],a[3]),pk2(b[0],b[1]),pk2(b[2],b[3])};
            *(LAS u32x4*)(xd+i*4096)=xv[9*h+i]; }
          if constexpr(h==0) var(0);
          SB(); }); }
      wait_lgkm0(); bar();
      auto ask=[&](){ gld4m<256>(qv,lane*4,ws+WS_Q,mk); };
      auto echo=[&](){ gst4m(u32x2{qv[0],nb},u32x2{qv[1],nb},u32x2{qv[2],nb},u32x2{qv[3],nb},lane*8,ws+WS_A,mk); };
      pin(pg[0]); pin(pg[1]); pin(pg[2]); pin(pg[3]);
      if(mk){ nb=(__builtin_amdgcn_readfirstlane(pg[0])+8u)&~7u;
        while(__any((pg[0]^nb)<8u||(pg[1]^nb)<8u||(pg[2]^nb)<8u||(pg[3]^nb)<8u)) nb+=8u; }
      ask();
      bar();
      dma(4); var(1);
      bar();
      dma(5);
      bar();
      dma(6); wait_vm<10>(); wait_vm4m<10>(qv,mk); echo(); ask();
      bar();
      dma(7); wait_vm<10>();
      bar();
      dma(8); wait_vm<10>();
      unsigned ct[2], cp2=0, cq2=0;
      {
        const int c=w*81+lane, c2=c+(lane<17?64:0); const char* m=((xcd&1)?wu:wg)+(size_t)ii*18*(F*2);
        auto off=[&](int e){ const int row=e/18, l=e-row*18; return (unsigned)(row*(F*2)+(l<16?(xcd>>1)*2048+l*128:8192+(xcd>>1)*256+(l-16)*128)); };
        gld1(ct[0],off(c),m); gld1(ct[1],off(c2),m); }
      bar();
      ldq(0,3); wait_vm<10>();
      bar();
      wait_vm<5>(); wait_lgkm0();
      bar();
      wait_vm4m<5>(qv,mk); echo();
      ldq(1,8);
      if(w<3){ if(ii<18){ const int u=(xcd&1)*18+ii; gld1(cp2,((u&3)*144+w*48+(lane<48?lane:0))*1152,wo+(u>>2)*128); }
        if(w<2) gld1(cq2,((ii&3)*48+(lane<48?lane:0))*(F*2),(w?wu:wg)+(8*(xcd>>1)+(ii>>2))*256); }
      const float rs=*(LAS float*)(lds+RS+tl*4);
      ldpw();
      if(w==0&&lane==0) *(LAS u32x2*)(lds+QC)=u32x2{0u,0u};
      wait_lgkm0(); bar();
      gld2c(av,0u,ws+WS_A+bx*8);
      {
        f32x16 kb={}; bf16x8 a[4],b[4];
        #pragma unroll
        for(int s=0;s<4;s++){ s16x4 ta[2],tb[2];
          #pragma unroll
          for(int t=0;t<2;t++){ const int sx=((q>>1)<<2)|(q&1)|(t<<1);
            LAS char* rp=lds+TI+(kw*64+16*s+8*hh+4*t+q)*128+(p&1)*8;
            ta[t]=trd(rp+(((4+sg)^sx)*16)); tb[t]=trd(rp+((((5+sg)&7)^sx)*16)); }
          a[s]=cat(ta[0],ta[1]); b[s]=cat(tb[0],tb[1]); }
        SB();
        #pragma unroll
        for(int s=0;s<4;s++) kb=MFMA32(a[s],b[s],kb);
        const int rho=r-2*(kw&1), row=(r>=20)?r-2:rho;
        if(hh==0 && (r==20 || r==21 || (rho>=0&&rho<18))) gstc2(r==21?u32x2{0u,0u}:u32x2{pk2(kb[0],kb[1]),pk2(kb[2],kb[3])},row*8,gd+1216); }
      ldq(2,0); ldq(3,1); ldq(4,2);
      wait_vm<9>(); pin(av); pin(ct[0]); pin(ct[1]); pin(cp2); pin(cq2);
      if(w==0){
        if(bx!=0){ unsigned long long a=((unsigned long long)av[1]<<32)|av[0];
          for(unsigned spins=0;(unsigned)a!=req&&spins<(1u<<18);spins++){ __builtin_amdgcn_s_sleep(1); a=__hip_atomic_load((const GAS unsigned long long*)(ws+WS_A)+bx,RLX_AGENT); }
          nb=(unsigned)(a>>32); }
        *(LAS unsigned*)(lds+BW)=nb; }
      wait_lgkm0(); bar();
      const unsigned aQ0=lb+RB+(8*hh+q)*144+72*hp+32*(p&1)+8*sg, aQ1=aQ0-8*sg+32, aQ0h=aQ0+36864, aQ1h=aQ1+36864;
      f32x16 c0={},c1={}; bf16x8 fa[PD+1],fb[PD+1],fc[PD+1];
      static_for<0,36+PD>([&](auto SS){ constexpr int s=decltype(SS)::value, m=s-PD, kg=s>>2, gp=(kg==0)?3:(kg==1)?8:(kg<5)?kg-2:kg-1, o=(gp&3)*9216+(gp==8?36864:0)+(s&3)*2304;
        if constexpr(s==8){ wait_vm<12>(); meet4((LAS unsigned*)(lds+QC)); }
        if constexpr(s==20){ wait_vm<0>(); meet4((LAS unsigned*)(lds+QC+4)); }
        if constexpr(s<36){ fa[s%(PD+1)]=ldtr<576>(((gp>=4)?aQ0h:aQ0)+o); fc[s%(PD+1)]=ldtr<576>(((gp>=4)?aQ1h:aQ1)+o); fb[s%(PD+1)]=*(LAS bf16x8*)(size_t)(aB[s&3]+s*1024); SB(); }
        if constexpr(m>=0){ c0=MFMA32(fa[m%(PD+1)],fb[m%(PD+1)],c0); c1=MFMA32(fc[m%(PD+1)],fb[m%(PD+1)],c1); SB(); }
        if constexpr(s>=3&&s<7){ ldq(s+2,s+1); SB(); }
      });
      base=__builtin_amdgcn_readfirstlane(*(LAS unsigned*)(lds+BW));
      unsigned ph[20]; float s1,s2;
      { float e1[18],e2[18]; feat(c0,c1,rs,e1,e2,s1,s2);
        const float a2=s1*__builtin_amdgcn_rcpf(s2);
        #pragma unroll
        for(int i=0;i<9;i++){ ph[i]=pk2(e1[2*i],e1[2*i+1]); ph[9+i]=pk2(e2[2*i]*a2,e2[2*i+1]*a2); }
        ph[18]=0u; ph[19]=0u; }
      const bool mine=((r>>2)&1)==hh;
      const LAS char* k0=lds+KVI+(mine?((2*hp+hh)*19+4*(r>>3)+(r&3))*80:6080);
      const LAS char* k1=lds+KVI+((mine&&r<8&&(r&3)<3)?((2*hp+hh)*19+16+(r&3))*80:6080);
      bar();
      f32x16 o0={},o1={};
      #pragma unroll
      for(int kp=0;kp<5;kp++){ const bf16x8 b=BF8((u32x4{ph[4*kp],ph[4*kp+1],ph[4*kp+2],ph[4*kp+3]}));
        o0=MFMA32(*(const LAS bf16x8*)(k0+kp*16),b,o0); o1=MFMA32(*(const LAS bf16x8*)(k1+kp*16),b,o1); }
      const float id=__builtin_amdgcn_rcpf(fmaf(s1,1e-6f,o1[2]));
      gstc(u32x4{pk2(o0[0]*id,o0[1]*id),pk2(o0[2]*id,o0[3]*id),pk2(o0[4]*id,o0[5]*id),pk2(o0[6]*id,o0[7]*id)},l16,dst+(2*pr)*1024);
      gstc(u32x4{pk2(o0[8]*id,o0[9]*id),pk2(o0[10]*id,o0[11]*id),pk2(o0[12]*id,o0[13]*id),pk2(o0[14]*id,o0[15]*id)},l16,dst+(2*pr+1)*1024);
      { const unsigned t=pk2(o1[0]*id,o1[1]*id); const auto sw=__builtin_amdgcn_permlane32_swap(t,t,false,false);
        if(hh==0) gstc2(u32x2{sw[0],sw[1]},l16,dst+(32+(pr>>2))*1024+((pr>>1)&1)*512+(pr&1)*8); }
      wait_vm<0>();
    } else {
      { const char* ps=wphi+(2*hg+hp)*1296;
        #pragma unroll
        for(int i=0;i<6;i++){ const unsigned c=(i<5?i*64:296)+lane, row=(c*205u)>>11, cc=c-row*10; glds4(ps,row*36+(cc<9?cc:8)*4,lb+KVI+kw*PHS+(i<5?i*256:1184)); }
      }
      dma(2); dma(3);
      wait_vm<10>(); pin(cp); ldpw();
      unsigned kd[3];
      #pragma unroll
      for(int c=0;c<3;c++){ const int g=2*(32*c+(lane&15)+16*(lane>>5))+((lane>>4)&1), ty=g>=76, sl=(g-76*ty)>>1;
        kd[c]=g<171?lb+KVI+kw*1520+(g<152?(sl>>1)*80+(4*ty+2*(g&1)+(sl&1))*8:(g-152)*80+64):0u; }
      u32x4 sel; { const unsigned o=(((lane>>3)^(lane>>4))&1)?0u:((lane&1)?0x3f800000u:0x00003f80u); const int j=(lane&7)>>1; sel=u32x4{j==0?o:0u,j==1?o:0u,j==2?o:0u,j==3?o:0u}; }
      pin(kd[0]); pin(kd[1]); pin(kd[2]); pin(sel);
      const unsigned aA0=ab+8*sg, aA1=ab+(sg==0?32:160+8*sg), aA2=ab+160+(sg==0?0:32), aA0h=aA0+8*SL, aA1h=aA1+8*SL, aA2h=aA2+8*SL;
      bar();
      f32x16 c0={},c1={},c2={}; bf16x8 fa0[PD+1],fa1[PD+1],fa2[PD+1],fb[PD+1];
      static_for<0,36+PD>([&](auto SS){ constexpr int s=decltype(SS)::value, m=s-PD;
        if constexpr(s>0&&s<36&&(s&3)==0){
          if constexpr(s==8) wait_vm<5>();
          if constexpr(s==12) wait_vm<0>();
          if constexpr(s!=28) wait_lgkm0();
          bar(); }
        if constexpr(s<36){ fb[s%(PD+1)]=*(LAS bf16x8*)(size_t)(aB[s&3]+s*1024); fa0[s%(PD+1)]=ldtr<1280>(((s&8)?aA0h:aA0)+(s&7)*SL); SB(); }
        if constexpr(m>=0){ c0=MFMA32(fa0[m%(PD+1)],fb[m%(PD+1)],c0); SB(); }
        if constexpr(s<36){ fa1[s%(PD+1)]=ldtr<1280>(((s&8)?aA1h:aA1)+(s&7)*SL); SB(); }
        if constexpr(m>=0){ c1=MFMA32(fa1[m%(PD+1)],fb[m%(PD+1)],c1); SB(); }
        if constexpr(s<36){ fa2[s%(PD+1)]=ldtr<1280>(((s&8)?aA2h:aA2)+(s&7)*SL); SB(); }
        if constexpr(m>=0){ c2=MFMA32(fa2[m%(PD+1)],fb[m%(PD+1)],c2); SB(); }
      });
      { const float rs=*(LAS float*)(lds+RS+tl*4);
        float e1[18],e2[18],s1,s2; feat(c0,c1,rs,e1,e2,s1,s2);
        *(LAS u32x4*)(trow+((5^sig)*16))=u32x4{pk2(c2[0],c2[1]),pk2(c2[2],c2[3]),pk2(c1[4],c1[5]),pk2(c1[6],c1[7])};
        *(LAS u32x4*)(trow+((7^sig)*16))=u32x4{pk2(c2[4],c2[5]),pk2(c2[6],c2[7]),pk2(__builtin_amdgcn_rcpf(rs),0.f),0u};
        *(LAS u32x4*)(trow+((6^sig)*16))=__builtin_bit_cast(u32x4,pack8(c1,8));
        const float a1=rs*__builtin_amdgcn_rcpf(s1), a2=rs*__builtin_amdgcn_rcpf(s2); unsigned ph[20];
        #pragma unroll
        for(int i=0;i<9;i++){ ph[i]=pk2(e1[2*i]*a1,e1[2*i+1]*a1); ph[9+i]=pk2(e2[2*i]*a2,e2[2*i+1]*a2); }
        ph[18]=0u; ph[19]=0u;
        #pragma unroll
        for(int i=0;i<5;i++) *(LAS u32x4*)(trow+((i^sig)*16))=u32x4{ph[4*i],ph[4*i+1],ph[4*i+2],ph[4*i+3]}; }
      wait_lgkm0(); bar();
      const int rt=ii&28, tq=(r+rt)&31, who=((tq&7)*4+(hg&3))*8+(tq>>3)*2+(hg>>2);
      {
        f32x16 ka={}; bf16x8 a[4],b[4];
        #pragma unroll
        for(int s=0;s<4;s++){ s16x4 ta[2],tb[2];
          #pragma unroll
          for(int t=0;t<2;t++){ const int sx=((q>>1)<<2)|(q&1)|(t<<1);
            LAS char* rp=lds+TI+(kw*64+16*s+8*hh+4*t+q)*128+(p&1)*8;
            ta[t]=trd(rp+((sg^sx)*16)); tb[t]=trd(rp+((((5+sg)&7)^sx)*16)); }
          a[s]=cat(ta[0],ta[1]); b[s]=cat(tb[0],tb[1]); }
        { LAS u32x4* z=(LAS u32x4*)(lds+KVI)+kw*95+lane; z[0]=u32x4{0u,0u,0u,0u}; if(lane<(kw==3?36:31)) z[64]=u32x4{0u,0u,0u,0u}; }
        SB();
        #pragma unroll
        for(int s=0;s<4;s++)ka=MFMA32(a[s],b[s],ka);
        const int rho=r-2*(kw&1), row=(r>=20)?r-2:rho;
        if(r==20 || (rho>=0&&rho<18)){ gstc(__builtin_bit_cast(u32x4,pack8(ka,0)),row*32+hh*16,gd); gstc(__builtin_bit_cast(u32x4,pack8(ka,8)),row*32+hh*16,gd+608); }
        }
      wait_vm<0>();
      bar();
      base=__builtin_amdgcn_readfirstlane(*(LAS unsigned*)(lds+BW));
      if(w==7&&lane==0) publish(ws,bx,base+1);
      {
        const char* pb=ws+WS_KVP+(size_t)hg*32*KVP+kw*1408;
        const unsigned g0=r*16+hh*KVP, g2_=(r<22?r:21)*16+hh*KVP+1024;
        constexpr int NW=10; u32x4 pv[NW][3]; f32x4 ac[3]={};
        auto use=[&](auto I,auto N){ constexpr int i=decltype(I)::value%NW; wait_vm3<decltype(N)::value>(pv[i][0],pv[i][1],pv[i][2]);
          ac[0]=MFMA16(BF8(sel),BF8(pv[i][0]),ac[0]); ac[1]=MFMA16(BF8(sel),BF8(pv[i][1]),ac[1]); ac[2]=MFMA16(BF8(sel),BF8(pv[i][2]),ac[2]); SB(); };
        if(bx==0&&w==7) wait_progress(ws,who,32,base,1,true);
        unsigned rdy=0;
        static_for<0,8>([&](auto G){ constexpr int g=decltype(G)::value;
          for(unsigned spins=0;(~rdy&(unsigned)((16ull<<(4*g))-1))!=0u&&spins<(1u<<18);spins++){
            if(spins) __builtin_amdgcn_s_sleep(2);
            rdy=(unsigned)__ballot(__hip_atomic_load((const GAS unsigned*)(ws+WS_FLAG)+who*32,RLX_AGENT)-base-1u<7u); }
          static_for<2*g,2*g+2>([&](auto I){ constexpr int i=decltype(I)::value;
            if constexpr(i>=NW) use(IC(i-NW),IC(3*NW-3));
            const char* s=pb+(size_t)((2*i+rt)&31)*KVP; gldc(pv[i%NW][0],g0,s); gldc(pv[i%NW][1],g0,s+512); gldc(pv[i%NW][2],g2_,s); SB(); }); });
        static_for<16-NW,16>([&](auto I){ use(I,IC(3*(15-decltype(I)::value))); });
        #pragma unroll
        for(int c=0;c<3;c++) if(kd[c]) *(LAS u32x2*)(size_t)kd[c]=u32x2{pk2(ac[c][0],ac[c][1]),pk2(ac[c][2],ac[c][3])}; }
      wait_lgkm0(); bar();
      if(ii<18){
        const int np=((xcd&1)*18+ii)>>2; const unsigned co=((lane&7)^(((lane>>4)&1)<<2))*16;
        #pragma unroll
        for(int v=0;v<2;v++){ const int rl=8*(kw+4*v)+(lane>>3), hk=(rl>>3)&1, j=rl&7;
          #pragma unroll
          for(int kb=0;kb<9;kb++){ const int s0=kb*4+((kw+4*v)>>1), h2=s0>=18, s1=s0-18*h2+2*np, s=18*h2+(s1>=18?s1-18:s1);
            const int row=(s<32)?36*(s>>1)+8*(s&1)+18*hk+j:144*(s-32)+16+72*hk+36*(j>>2)+18*((j>>1)&1)+(j&1);
            glds16(wo+np*128,row*1152+co,lb+RB+kb*8192+(kw+4*v)*1024); } } }
    }
    bar();
    if(w==0 && lane==0) publish(ws,bx,base+2);
  }
  const char* gA=wg+(8*(xcd>>1)+(ii>>2))*256; const char* uA=wu+(8*(xcd>>1)+(ii>>2))*256;
  asm volatile("":"+s"(gA),"+s"(uA));
  auto p3pre=[&](){
    const int lane=lane_id();
    const unsigned voffS=(unsigned)((8*w+(lane>>3))*(F*2)+(((lane&7)^(((lane>>4)&1)<<2))*16));
    #pragma unroll
    for(int kb=0;kb<3;kb++){ glds16(gA+(size_t)kb*64*F*2,voffS,lb+kb*16384+w*1024); glds16(uA+(size_t)kb*64*F*2,voffS,lb+kb*16384+8192+w*1024); } };
  if(ii<18){
    const int lane=lane_id(), hh=lane>>5, r=lane&31; const unsigned l16=lane*16;
    const int u=(xcd&1)*18+ii, np=u>>2, tk=4*(xcd>>1)+(u&3), tg=w&3, kh=w>>2;
    if(w==0) wait_progress(ws,(((2*tk+((lane>>3)&1))&7)*4+(lane&3))*8+(xcd&6)+((lane>>2)&1),16,base,2,bx==0);
    wait_vm<0>(); bar();
    u32x4 ov[18], xr[2];
    const char* os=ws+WS_O+((size_t)(tk*4+tg)*36+kh*18)*1024;
    auto ldo=[&](int i){ const int s=i+2*np; gldc(ov[i],l16,os+(s>=18?s-18:s)*1024); };
    {
      #pragma unroll
      for(int i=0;i<8;i++) ldo(i); }
    const int q=(lane&15)>>2, p=lane&3, g2=(lane>>4)&1;
    const unsigned a0_=(8*hh+q)*128+(((2*g2+(p>>1))^((q>>1)<<2))*16)+(p&1)*8, a0=lb+73728+kh*18*2048+a0_, a1=lb+73728+kh*18*2048+(a0_^64);
    f32x16 c0={},c1={};
    static_for<0,9>([&](auto G){ constexpr int g=decltype(G)::value;
      const bf16x8 f0=ldtr<512>(a0+g*4096), f1=ldtr<512>(a1+g*4096), f2=ldtr<512>(a0+g*4096+2048), f3=ldtr<512>(a1+g*4096+2048);
      wait_vm2<(g<7?6:16+4-2*g-2)>(ov[2*g],ov[2*g+1]);
      c0=MFMA32(f0,BF8(ov[2*g]),c0); c1=MFMA32(f1,BF8(ov[2*g]),c1); SB();
      if constexpr(g<5){ ldo(2*g+8); ldo(2*g+9); }
      if constexpr(g==5){ const char* xs=x+((size_t)(tk*128+tg*32)*576+np*64+kh*32)*2; gld(xr[0],r*1152+hh*16,xs); gld(xr[1],r*1152+hh*16,xs+32); }
      c0=MFMA32(f2,BF8(ov[2*g+1]),c0); c1=MFMA32(f3,BF8(ov[2*g+1]),c1); SB(); });
    {
      LAS char* sx=lds+(w<6?49152+w*4096:147456+(w-6)*4096)+l16; LAS char* rx=lds+((w^4)<6?49152+(w^4)*4096:147456+((w^4)-6)*4096)+l16;
      #pragma unroll
      for(int i=0;i<4;i++) *(LAS u32x4*)(sx+i*1024)=kh?u32x4{__float_as_uint(c0[4*i]),__float_as_uint(c0[4*i+1]),__float_as_uint(c0[4*i+2]),__float_as_uint(c0[4*i+3])}
                                                       :u32x4{__float_as_uint(c1[4*i]),__float_as_uint(c1[4*i+1]),__float_as_uint(c1[4*i+2]),__float_as_uint(c1[4*i+3])};
      wait_lgkm0(); bar();
      #pragma unroll
      for(int i=0;i<4;i++){ const u32x4 v=*(LAS u32x4*)(rx+i*1024);
        #pragma unroll
        for(int e=0;e<4;e++) c0[4*i+e]=(kh?c1[4*i+e]:c0[4*i+e])+__uint_as_float(v[e]); } }
    wait_vm2<0>(xr[0],xr[1]);
    unsigned o[8]; float v[16], sm=0.f;
    #pragma unroll
    for(int i=0;i<2;i++){ const auto x0=__builtin_amdgcn_permlane32_swap(xr[i][0],xr[i][2],false,false), x1=__builtin_amdgcn_permlane32_swap(xr[i][1],xr[i][3],false,false);
      #pragma unroll
      for(int e=0;e<2;e++){ const int n=4*(2*i+e);
        o[4*i+2*e]=pk2(c0[n]+lo16(x0[e]),c0[n+1]+hi16(x0[e])); o[4*i+2*e+1]=pk2(c0[n+2]+lo16(x1[e]),c0[n+3]+hi16(x1[e])); } }
    char* dst=ws+WS_H+((size_t)(tk*4+tg)*36+np*4+kh*2)*1024;
    gstc(u32x4{o[0],o[1],o[2],o[3]},l16,dst); gstc(u32x4{o[4],o[5],o[6],o[7]},l16,dst+1024);
    #pragma unroll
    for(int i=0;i<8;i++){ v[2*i]=lo16(o[i]); v[2*i+1]=hi16(o[i]); sm+=v[2*i]+v[2*i+1]; }
    sm=xsum32(sm)*(1.f/32);
    float qq=0.f;
    #pragma unroll
    for(int i=0;i<16;i++){ const float d=v[i]-sm; qq+=d*d; }
    qq=xsum32(qq);
    if(hh==0) gstc2(u32x2{__float_as_uint(sm),__float_as_uint(qq)},r*16+kh*8,ws+WS_ST+((size_t)np*S+tk*128+tg*32)*16);
    wait_vm<0>(); bar();
    if(w==0 && lane==0) publish(ws,bx,base+3);
    p3pre();
  } else if(ii<27){
    p3pre();
    const int lane=lane_id();
    const int m=xcd&1, cb=(ii<26)?8*(xcd>>1)+ii-18:32+(xcd>>1);
    const char* src=(m?wu:wg)+cb*256+(size_t)w*72*(F*2);
    float a[8]={0.f,0.f,0.f,0.f,0.f,0.f,0.f,0.f};
    u32x4 v[18];
    #pragma unroll
    for(int i=0;i<18;i++) gld(v[i],(lane>>4)*(F*2)+(lane&15)*16,src+(size_t)i*4*(F*2));
    wait_vm<0>();
    #pragma unroll
    for(int i=0;i<18;i++){ pin(v[i]);
      #pragma unroll
      for(int e=0;e<4;e++){ a[2*e]+=lo16(v[i][e]); a[2*e+1]+=hi16(v[i][e]); } }
    #pragma unroll
    for(int e=0;e<8;e++){ a[e]+=__shfl_xor(a[e],16); a[e]+=__shfl_xor(a[e],32); }
    LAS float* red=(LAS float*)(lds+65536);
    if(lane<16){
      #pragma unroll
      for(int e=0;e<8;e++) red[w*128+lane*8+e]=a[e]; }
    wait_lgkm0(); bar();
    if(w<2){ const int t_=w*64+lane; float s=0.f;
      #pragma unroll
      for(int i=0;i<8;i++) s+=red[i*128+t_];
      const unsigned hi=pk2(s,s)&0xffffu, lo=pk2(s-lo16(hi),0.f)&0xffffu;
      const int f=cb*128+t_; int j,t,l;
      if(f<4096){ j=f>>7; t=((f>>5)&3)*2+m; l=f&31; } else { j=(f-4096)>>4; t=8; l=(f&15)+16*m; }
      gstc1(hi|(lo<<16),(((j*3+(t>>2))*32+l)*4+(t&3))*4,ws+WS_CS); wait_vm<0>(); }
    bar();
    if(w==0 && lane==0) publish(ws,bx,base+3);
  } else {
    p3pre();
    const int lane=lane_id(), sk=xcd>>1;
    u32x4 v[16];
    #pragma unroll
    for(int i=0;i<16;i++){ const int o=((((xcd&1)*5+ii-27)*8+w)*16+i)*1024;
      gld(v[i],lane*16,wd+(o<1024*1152?(size_t)sk*1024*1152+o:(size_t)(4096+128*sk)*1152+(o-1024*1152))); }
    wait_vm<0>();
    #pragma unroll
    for(int i=0;i<16;i++) pin(v[i]);
  }
  const int sk=xcd>>1, t4=8*(xcd&1)+(ii&7), ng=ii>>3;
  unsigned vW4, pl4, tW4, tH4, oW4=0, oH4=0;
  auto mk4=[&](){ const int l=lane_id(); vW4=(unsigned)((8*(w&1)+(l>>3))*(D*2)+(((l&7)^(((l>>4)&1)<<2))*16));
    auto fs=[&](int i){ const int c=l-12+3*(ng&1)+(ng&2), c6=c>=6?c-6:c;
      return l<8?64*sk+8*((5*ng+l)&7)+i:(l<12||c6<4)?64*sk+16*(l<12?(l+ng)&3:c6)+8*(i>>1)+(l<12?4:6)+(i&1):240+8*sk+4*c6+i; };
    tW4=fs(w>>1)*(16*D*2); tH4=fs(2*(w>>2))*1024; };
  const char* w4a=wd+ng*256; const char* w4e=wd+1024; const char* h4=ws+WS_G+(size_t)(t4*4+(w&3))*288*1024;
  const unsigned l4w=lb+w*1024, l4h=lb+16384+((w&3)*4+2*(w>>2))*1024;
  auto ld4=[&](auto P,auto O,unsigned l16){ constexpr int p=decltype(P)::value, o=decltype(O)::value;
    if constexpr(o==0){ oH4=addlane<p>(tH4,l16); gldsx<slot4(p),0,true>(h4,oH4,l4h); }
    if constexpr(o==1) gldsx<slot4(p)+1024,1024,true>(h4,oH4,l4h);
    if constexpr(o==2){ oW4=addlane<p>(tW4,vW4); gldsx<slot4(p),0,false>(w4a,oW4,l4w); }
    if constexpr(o==3) gldsx<slot4(p)+8192,128,false>(w4a,oW4,l4w);
    if constexpr(o==4) gldsx<p==13?147456:106496,0,false>(w4e,oW4,l4w); };
  auto ask4=[&](){ gld1c(pl4,(((lane_id()&7)*4+((t4>>1)&3))*8+xcd)*128,ws+WS_FLAG); };
  auto got4=[&](unsigned want){ pin(pl4); if(!__all(pl4-base-want<8u-want)) wait_progress(ws,((lane_id()&7)*4+((t4>>1)&3))*8+xcd,8,base,want); };
  {
    const int lane=lane_id();
    const int tt=4*(xcd&1)+(ii&3), j=8*(xcd>>1)+(ii>>2), T=tt*8+w;
    const unsigned voffS=(unsigned)((8*w+(lane>>3))*(F*2)+(((lane&7)^(((lane>>4)&1)<<2))*16));
    const unsigned xoff=lane*16; const char* xb=ws+WS_H+(size_t)T*36*1024;
    u32x4 x_[36], sp[5], cv[3];
    auto ldw=[&](int m,int kb,int ch,int ps){ glds16((m?uA:gA)+ch*128+(size_t)kb*64*F*2, voffS, lb+ps*16384+m*8192+w*1024); };
    if(w==0){
      int who; if(lane<18){ const int u=(lane>>1)*4+((2*tt+(lane&1))&3); who=(u%18)*8+(tt>>1)*2+u/18; } else who=((lane&2)?26:18+(j&7))*8+(xcd&6)+(lane&1);
      wait_progress(ws,who,22,base,3); }
    bar();
    #pragma unroll
    for(int i=0;i<4;i++) gldc(x_[i],xoff,xb+i*1024);
    bar();
    #pragma unroll
    for(int i=4;i<8;i++) gldc(x_[i],xoff,xb+i*1024);
    auto lds_=[&](int i){ const unsigned l16=(lane_id()&31)*16;
      if(i<4) gldc(sp[i],l16+(lane_id()>>5)*(4*S*16),ws+WS_ST+(size_t)T*32*16+i*(S*16)); else if(i==4) gldc(sp[4],l16,ws+WS_ST+(size_t)T*32*16+8*S*16); else gldc(cv[i-5],l16,ws+WS_CS+j*1536+(i-5)*512); };
    if(w>=4) __builtin_amdgcn_s_setprio(1);
    const int q=(lane&15)>>2, p=lane&3, g2=(lane>>4)&1, hh=lane>>5;
    const unsigned a0_=(4*hh+q)*128+(((2*g2+(p>>1))^((q>>1)<<2))*16)+(p&1)*8, a0=lb+a0_, a1=lb+(a0_^64);
    unsigned ac=0;
    auto ldc=[&](int pi,int sl){ const unsigned l4=lane_id(); const int row0=pi*128+16*w-((pi==4&&w>=4)?64:0);
      const char* m=((l4&2)?wu:wg)+(size_t)(4096+16*j)*2+(size_t)row0*(F*2);
      glds16v(m+((l4>>2)*(F*2)+(l4&1)*16), lb+sl*8192+w*1024); };
    f32x16 acc[4]={};
    char* outb=ws+WS_G+(size_t)T*288*1024;
    constexpr int PD=4, NU=324;
    bf16x8 A[PD]; u32x4 eo, bm; float k1, iv, sm;
    auto unit=[](int u,int& kb,int& s,int& t){
      if(u<96){ kb=u>>4; s=(u>>2)&3; t=u&3; }
      else if(u<120){ int v=u-96; kb=6+(v>>3); s=(v>>1)&3; t=v&1; }
      else if(u<144){ int v=u-120; kb=6+(v>>3); s=(v>>1)&3; t=2+(v&1); }
      else if(u<216){ int v=u-144; kb=v>>3; s=(v>>1)&3; t=v&1; }
      else if(u<288){ int v=u-216; kb=v>>3; s=(v>>1)&3; t=2+(v&1); }
      else { int v=u-288; kb=v>>2; s=v&3; t=0; } };
    unsigned pb0=0,pb1=0;
    auto rd=[&](auto U){ constexpr int u=decltype(U)::value; int kb=0,s=0,t=0; unit(u,kb,s,t);
      if constexpr(u<288){ constexpr int kbc=(u<96)?(u>>4):(u<120)?6+((u-96)>>3):(u<144)?6+((u-120)>>3):(u<216)?((u-144)>>3):((u-216)>>3); constexpr int ps=(u<144)?kbc:(kbc+9)%10;
        if constexpr(u<96){ if constexpr((u&15)==0) pb0=addc<ps*16384>(a0); if constexpr((u&15)==2) pb1=addc<ps*16384>(a1); }
        else if constexpr((u&7)==0){ if constexpr(u<120||(u>=144&&u<216)) pb0=addc<ps*16384>(a0); else pb1=addc<ps*16384>(a1); }
        A[u%PD]=ldtr<1024>(((t&2)?pb1:pb0)+(t&1)*8192+s*2048); }
      else { constexpr int v=u-288, pi=v/8, sl=(pi<4)?16+pi:0;
        if constexpr(v==0){ const int l=lane_id(); ac=lb+(4*(l>>5)+((l&15)>>2))*64+(2*((l>>4)&1)+((l&3)>>1))*16+(l&1)*8; }
        if constexpr((v&7)==0) pb0=addc<sl*8192>(ac);
        A[u%PD]=ldtr<512>(pb0+(v%8)*1024); } };
    float ea[2], eb[2], ep;
    auto job=[&](auto I,auto NE,const f32x16& g,const f32x16& u,int uo,int fs){ constexpr int i=decltype(I)::value, ne=decltype(NE)::value;
      constexpr int k=i/3, st=i%3, e=k-st;
      if constexpr(e>=0 && e<ne){
        if constexpr(st==0) ea[e&1]=__builtin_amdgcn_exp2f(g[e]*k1);
        if constexpr(st==1) eb[e&1]=__builtin_amdgcn_rcpf(fmaf(ea[e&1],iv,iv));
        if constexpr(st==2){ float v=opf(g[e]*u[uo+e]*eb[e&1]);
          if constexpr((e&1)==0) ep=v; else eo[(e>>1)&3]=pk2(ep,v);
          if constexpr((e&7)==7) gstc(eo,xoff,outb+(fs+(e>>3))*1024); } } };
    auto spread=[&](auto U,auto U0,auto L,const f32x16& g,const f32x16& uu,int fs){ constexpr int u=decltype(U)::value,u0=decltype(U0)::value,l=decltype(L)::value;
      if constexpr(u>=u0 && u<u0+l){ constexpr int b=(u-u0)*54/l, e=(u-u0+1)*54/l; static_for<b,e>([&](auto I){ job(I,IC(16),g,uu,0,fs); }); } };
    auto corr=[&](unsigned d,const f32x16& c){ u32x4 a={d,d&0xffffu,0u,0u}; return MFMA32(BF8(a),BF8(bm),c); };
    static_for<-PD,NU>([&](auto U){ constexpr int u=decltype(U)::value; constexpr int r=u+PD;
      if constexpr(r<=96 && (r&15)==0){ constexpr int kb=r>>4;
        constexpr int n=(kb==0)?4:(kb==1)?8:(kb==2)?12:(kb==3)?14:(kb==4)?11:(kb==5)?12:14;
        wait_vm4<n>(x_[4*kb],x_[4*kb+1],x_[4*kb+2],x_[4*kb+3]);
        bar(); }
      if constexpr(r==104){ wait_vm4<11>(x_[28],x_[29],x_[30],x_[31]); bar(); }
      if constexpr(r==112){ wait_vm4<6>(x_[32],x_[33],x_[34],x_[35]); bar(); }
      if constexpr(r==144){ wait_vm<3>(); bar(); }
      if constexpr(r==144+40){ wait_vm<2>(); bar(); }
      if constexpr(r==288){ wait_vm<1>(); bar(); }
      if constexpr(u>=0){ int kb=0,s=0,t=0; unit(u,kb,s,t);
        if constexpr(u==144||u==145) acc[u-144]=corr(cv[1][u-144],f32x16{});
        if constexpr(u==216||u==217) acc[u-214]=corr(cv[1][2+u-216],f32x16{});
        if constexpr(u==288) acc[0]=corr(cv[2][0],f32x16{});
        if constexpr(u>=88&&u<92) acc[u-88]=corr(cv[0][u-88],acc[u-88]);
        acc[t]=MFMA32(A[u%PD],BF8(x_[u<288?4*kb+s:u-288]),acc[t]);
        SB(); }
      if constexpr(r<NU) rd(IC(r));
      if constexpr(r<96){ if constexpr((r&1)==0){ constexpr int kb=r>>4, sl=(r&15)>>1;
          constexpr int op[6][8]={{8,9,10,11,-1,-2,12,13},{14,15,-1,-2,16,17,18,19},{40,41,42,43,44,-1,-2,45},{20,21,22,23,-1,-2,24,25},{26,27,-1,-2,28,29,30,31},{-1,-2,32,33,34,35,46,47}}, o=op[kb][sl];
          if constexpr(o<0) ldw(-1-o,kb+3,0,kb+3); else if constexpr(o<40) gldc(x_[o],xoff,xb+o*1024); else lds_(o-40); } }
      else if constexpr(r<128){ constexpr int o=(r-96)&7; constexpr int slot=(o==1)?0:(o==3)?1:(o==6)?2:-1;
        if constexpr(slot>=0){ constexpr int n=((r-96)>>3)*3+slot, i=n>>1; ldw(n&1,i,1,i==0?9:i-1); } }
      if constexpr(r==130) ldw(0,6,1,5);
      if constexpr(r==133) ldw(1,6,1,5);
      if constexpr(r==80) pin(cv[0]);
      if constexpr(r==112){ pin(cv[1]); pin(cv[2]); }
      if constexpr(r>=64 && r<76){
        if constexpr(r==64){ pin(sp[0]); pin(sp[1]); pin(sp[2]); pin(sp[3]); pin(sp[4]); }
        if constexpr(r==65){ const bool h2=lane_id()>=32; sp[4][0]=h2?sp[4][2]:sp[4][0]; sp[4][1]=h2?sp[4][3]:sp[4][1]; float a=__uint_as_float(sp[4][0]);
          #pragma unroll
          for(int i=0;i<4;i++) a+=__uint_as_float(sp[i][0])+__uint_as_float(sp[i][2]);
          sm=xsum32(a)*(1.f/18); }
        if constexpr(r==69){ float d=__uint_as_float(sp[4][0])-sm; float a=__uint_as_float(sp[4][1]), b=d*d;
          #pragma unroll
          for(int i=0;i<8;i++){ d=__uint_as_float(sp[i>>1][(i&1)*2])-sm; a+=__uint_as_float(sp[i>>1][(i&1)*2+1]); b=fmaf(d,d,b); }
          a=fmaf(b,32.f,a);
          iv=xsum32(a)*(1.f/D)+1e-5f; }
        if constexpr(r==74){ k1=__builtin_amdgcn_rsqf(iv)*-1.44269504f;
          const unsigned mh=pk2(-sm,-sm); const float ml=-sm-lo16(mh);
          const bool h2=lane_id()>=32; bm=u32x4{h2?0u:mh, h2?0u:(pk2(ml,ml)&0xffffu), 0u, 0u}; } }
      if constexpr(r==144+1) ldw(0,7,1,6);
      if constexpr(r==144+4) ldw(1,7,1,6);
      if constexpr(r==144+7) ldw(0,8,1,7);
      if constexpr(r==144+10) ldw(1,8,1,7);
      if constexpr(r==144+42) ldc(0,16);
      if constexpr(r==144+46) ldc(1,17);
      if constexpr(u==196){ const int e=(ii*8+w)*41+lane_id(), e2=(lane_id()<41&&e<10368)?e:0, row=e2/9;
        gld1(pl4,(row<1024?1024*sk+row:3072+128*sk+row)*(D*2)+(e2-row*9)*128,wd); }
      if constexpr(u==268){ wait_vm<1>(); pin(pl4); }
      if constexpr(u==216+15){ SB(); wait_vm<3>(); bar(); if(w==0 && lane_id()==0) publish(ws,bx,base+4); }
      if constexpr(u==270) ask4();
      if constexpr(u==216+16) ldc(2,18);
      if constexpr(u==216+19) ldc(3,19);
      if constexpr(u==216+22) ldc(4,0);
      if constexpr(u==289) mk4();
      if constexpr(u==290) got4(4);
      if constexpr(u>=290&&u<323&&((u-290)*2)%5<2){ constexpr int n=((u-290)*2)/5, p=n<5?0:(n-1)>>2, o=n<5?n:(n-1)&3; ld4(IC(p),IC(o),xoff); }
      spread(U,IC(121),IC(23),acc[0],acc[1],8*j);
      spread(U,IC(146),IC(34),acc[2],acc[3],8*j+2);
      spread(U,IC(218),IC(66),acc[0],acc[1],8*j+4);
      spread(U,IC(290),IC(34),acc[2],acc[3],8*j+6);
      SB();
    });
    static_for<0,30>([&](auto I){ job(I,IC(8),acc[0],acc[0],8,256+j); });
  }
  {
    const int lane=lane_id(); const unsigned l16=lane*16;
    const int tg=w&3, ch=w>>2;
    const int q=(lane&15)>>2, p=lane&3, g2=(lane>>4)&1, hh=lane>>5;
    const unsigned a0_=(4*hh+q)*128+(((2*g2+(p>>1))^((q>>1)<<2))*16)+(p&1)*8, a0=lb+ch*8192+a0_, a1=lb+ch*8192+(a0_^64), aE=lb+106496+(a0_^(ch*64)), aH=lb+16384+tg*4096+l16;
    f32x16 c[3]={};
    constexpr int PD=4, NU=164;
    bf16x8 A[PD], B[4]; unsigned pb0=0,pb1=0,pbH=0;
    const unsigned keep=ng<2?~0u:0u;
    static_for<-PD,NU>([&](auto U){ constexpr int u=decltype(U)::value, r=u+PD;
      constexpr int pr=stg(r), orr=r-st0(pr), nr=2+ex4(pr);
      if constexpr(r<NU && orr==0){
        constexpr int n=(pr<16?4+ex4(pr+2):0)+(pr<17?4+ex4(pr+1):0);
        wait_vm<n>(); bar();
        if constexpr(pr==0){ if(w==0 && lane==0) publish(ws,bx,base+5); }
        if constexpr(pr==2){ if(w==0 && lane==0) publish(ws,bx,base+6); }
        if constexpr(pr==5) got4(5);
        if constexpr(pr==9) got4(6);
        pb0=addc<slot4(pr)>(a0); pb1=addc<slot4(pr)>(a1); pbH=addc<slot4(pr)>(aH); }
      if constexpr(u>=0){ constexpr int pu=stg(u), ou=u-st0(pu), nu=2+ex4(pu), s=ou/nu, t=ou%nu;
        if constexpr(pu==13&&t==2){ u32x4 b=__builtin_bit_cast(u32x4,B[s]); b&=keep; c[2]=MFMA32(A[u%PD],BF8(b),c[2]); }
        else c[t]=MFMA32(A[u%PD],B[s],c[t]);
        SB(); }
      if constexpr(r<NU){ constexpr int s=orr/nr, t=orr%nr;
        if constexpr(t==0) B[s]=*(LAS bf16x8*)(size_t)(pbH+s*1024);
        A[r%PD]=ldtr<1024>((t==0?pb0:t==1?pb1:aE)+s*2048+((t==2&&pr==13)?40960:0)); }
      if constexpr(u==st0(14)+3||u==st0(14)+6){ constexpr int hf=u==st0(14)+6; gstc(__builtin_bit_cast(u32x4,pack8(c[2],8*hf)),l16,ws+WS_R+(size_t)((t4*16+tg*4+ch*2+hf)*16+sk*4+ng)*1024); }
      if constexpr(r<NU && pr+3<18){
        if constexpr((pr==3||pr==7)&&orr==0) ask4();
        if constexpr(orr==1||orr==3) ld4(IC(pr+3),IC(orr>>1),l16);
        if constexpr(orr==5||orr==6||(orr==7&&ex4(pr+3))) ld4(IC(pr+3),IC(orr-3),l16); }
      SB();
    });
    __builtin_amdgcn_s_setprio(0);
    char* xb=ws+WS_X+(size_t)((t4*4+ng)*4+tg)*4*8192+ch*4096;
    char* rb=ws+WS_R+(size_t)t4*256*1024;
    if(tg!=sk){ char* d=xb+sk*8192;
      gstc(__builtin_bit_cast(u32x4,pack8(c[0],0)),l16,d); gstc(__builtin_bit_cast(u32x4,pack8(c[0],8)),l16,d+1024);
      gstc(__builtin_bit_cast(u32x4,pack8(c[1],0)),l16,d+2048); gstc(__builtin_bit_cast(u32x4,pack8(c[1],8)),l16,d+3072);
    } else {
      #pragma unroll
      for(int i=0;i<8;i++) *(LAS f32x4*)(lds+73728+ch*8192+i*1024+l16)=f32x4{c[i>>2][(4*i)&15],c[i>>2][(4*i+1)&15],c[i>>2][(4*i+2)&15],c[i>>2][(4*i+3)&15]}; }
    u32x4 rr; gldc(rr,l16,ws+WS_H+((size_t)(t4*4+sk)*36+ng*8+w)*1024);
    wait_vm<0>(); wait_lgkm0(); bar();
    if(w==0 && lane==0) publish(ws,bx,base+7);
    const f32x4 m0=*(LAS f32x4*)(lds+73728+w*2048+l16), m1=*(LAS f32x4*)(lds+73728+w*2048+1024+l16);
    auto quarter=[&](){
      u32x4 pv[3];
      #pragma unroll
      for(int s=0;s<3;s++) gldc(pv[s],l16,ws+WS_X+(size_t)(((t4*4+ng)*4+sk)*4+s+(s>=sk?1:0))*8192+w*1024);
      wait_vm3<0>(pv[0],pv[1],pv[2]); pin(rr);
      float v[8];
      #pragma unroll
      for(int e=0;e<4;e++){
        v[2*e]=(e<2?m0[2*e]:m1[2*e-4])+lo16(rr[e])+lo16(pv[0][e])+lo16(pv[1][e])+lo16(pv[2][e]);
        v[2*e+1]=(e<2?m0[2*e+1]:m1[2*e-3])+hi16(rr[e])+hi16(pv[0][e])+hi16(pv[1][e])+hi16(pv[2][e]); }
      const auto s0=__builtin_amdgcn_permlane32_swap(pk2(v[0],v[1]),pk2(v[4],v[5]),false,false), s1=__builtin_amdgcn_permlane32_swap(pk2(v[2],v[3]),pk2(v[6],v[7]),false,false);
      gsto(u32x4{s0[0],s1[0],s0[1],s1[1]},(((t4*4+sk)*32+(lane&31))*D+ng*128+w*16+8*hh)*2,outp);
      };
    if(w!=4){ wait_progress(ws,ii*8+(lane&3)*2+(xcd&1),4,base,7); quarter(); }
    else {
      u32x2 hr[2];
      const int n=(lane&15)+16*hh, hf=(lane>>4)&1;
      { const char* hs=ws+WS_H+((size_t)(t4*4+sk)*36+32+ng)*1024; gld2c(hr[0],n*16+hf*8,hs); gld2c(hr[1],n*16+hf*8,hs+512); }
      wait_progress(ws,((lane>>2&3)*8+(ii&7))*8+(lane&3)*2+(xcd&1),16,base,7);
      u32x4 pv[8][2];
      { const char* d=rb+(sk*4+ng)*16384;
        #pragma unroll
        for(int i=0;i<8;i++){ gldc(pv[i][0],(lane&31)*16+hh*1024,d+i*2048); gldc(pv[i][1],(lane&31)*16+hh*1024,d+i*2048+512); } }
      u32x4 sel; { const unsigned o=(((lane>>3)^(lane>>4))&1)?0u:((lane&1)?0x3f800000u:0x00003f80u); const int j=(lane&7)>>1; sel=u32x4{j==0?o:0u,j==1?o:0u,j==2?o:0u,j==3?o:0u}; }
      quarter();
      f32x4 e0={},e1={};
      #pragma unroll
      for(int i=0;i<8;i++){ pin(pv[i][0]); pin(pv[i][1]); e0=MFMA16(BF8(sel),BF8(pv[i][0]),e0); e1=MFMA16(BF8(sel),BF8(pv[i][1]),e1); }
      pin(hr[0]); pin(hr[1]);
      gsto(u32x4{pk2(e0[0]+lo16(hr[0][0]),e0[1]+hi16(hr[0][0])),pk2(e0[2]+lo16(hr[0][1]),e0[3]+hi16(hr[0][1])),pk2(e1[0]+lo16(hr[1][0]),e1[1]+hi16(hr[1][0])),pk2(e1[2]+lo16(hr[1][1]),e1[3]+hi16(hr[1][1]))},
           (((t4*4+sk)*32+n)*D+512+ng*16+hf*8)*2,outp);
    }
  }
}

extern "C" void kernel_launch(void* const* d_in, const int* in_sizes, int n_in, void* d_out, int out_size, void* d_ws, size_t ws_size, hipStream_t stream){
  (void)hipFuncSetAttribute((const void*)mega,hipFuncAttributeMaxDynamicSharedMemorySize,163840);
  mega<<<256,512,163840,stream>>>((const char*)d_in[0],(const char*)d_in[1],(const char*)d_in[2],(const char*)d_in[3],(const char*)d_in[5],(const char*)d_in[6],(const char*)d_in[7],(char*)d_ws,(char*)d_out);
}
```

```cpp
#include <hip/hip_runtime.h>
#include <hip/hip_bf16.h>
#include <type_traits>
typedef unsigned short u16;
typedef __attribute__((ext_vector_type(4))) short s16x4;
typedef __attribute__((ext_vector_type(8))) __bf16 bf16x8;
typedef __attribute__((ext_vector_type(2))) __bf16 bf16x2;
typedef __attribute__((ext_vector_type(2))) float f32x2;
typedef __attribute__((ext_vector_type(2))) unsigned u32x2;
typedef __attribute__((ext_vector_type(4))) unsigned u32x4;
typedef __attribute__((ext_vector_type(4))) float f32x4;
typedef __attribute__((ext_vector_type(16))) float f32x16;
#define LAS __attribute__((address_space(3)))
#define GAS __attribute__((address_space(1)))
#define DI __device__ __forceinline__
#define RLX_AGENT __ATOMIC_RELAXED, __HIP_MEMORY_SCOPE_AGENT

constexpr unsigned CODE_PF=73728;
constexpr int S=2048, D=576, F=4608;

template<int I,int N,class Fn> DI void static_for(Fn&& f){ if constexpr(I<N){ f(std::integral_constant<int,I>{}); static_for<I+1,N>(f);} }
#define IC(n) std::integral_constant<int,(n)>{}
#define SB() __builtin_amdgcn_sched_barrier(0)
#define MFMA32(a,b,c) __builtin_amdgcn_mfma_f32_32x32x16_bf16((a),(b),(c),0,0,0)
#define MFMA16(a,b,c) __builtin_amdgcn_mfma_f32_16x16x32_bf16((a),(b),(c),0,0,0)
#define BF8(v) __builtin_bit_cast(bf16x8,(v))
DI unsigned pk2(float a,float b){ f32x2 f={a,b}; return __builtin_bit_cast(unsigned,__builtin_convertvector(f,bf16x2)); }
DI float lo16(unsigned u){ return __uint_as_float(u<<16); }
DI float hi16(unsigned u){ return __uint_as_float(u&0xffff0000u); }
DI float opf(float v){ asm("":"+v"(v)); return v; }
DI int lane_id(){ int l; asm volatile("v_mbcnt_lo_u32_b32 %0, -1, 0\n\tv_mbcnt_hi_u32_b32 %0, -1, %0":"=v"(l)); return l; }
template<int N> DI void wait_vm(){ asm volatile("s_waitcnt vmcnt(%0)"::"n"(N):"memory"); }
DI void wait_lgkm0(){ asm volatile("s_waitcnt lgkmcnt(0)":::"memory"); }
DI void bar(){ __builtin_amdgcn_s_barrier(); }
DI s16x4 trd(LAS char* p){ return __builtin_amdgcn_ds_read_tr16_b64_v4i16((LAS s16x4*)p); }
DI bf16x8 cat(s16x4 a,s16x4 b){ return BF8(__builtin_shufflevector(a,b,0,1,2,3,4,5,6,7)); }
template<int TS> DI bf16x8 ldtr(unsigned p){ return cat(trd((LAS char*)(size_t)p),trd((LAS char*)(size_t)p+TS)); }
template<int C> DI unsigned addc(unsigned v){ unsigned r; asm volatile("v_add_u32 %0, %1, %2":"=v"(r):"n"(C),"v"(v)); return r; }
DI void glds16(const char* sb, unsigned voff, unsigned ldsaddr){
  asm volatile("s_mov_b32 m0, %2\n\ts_nop 0\n\tglobal_load_lds_dwordx4 %0, %1"::"v"(voff),"s"(sb),"s"(ldsaddr):"memory"); }
template<int C,int OFF,bool SC> DI void gldsx(const char* sb, unsigned voff, unsigned lw){
  if constexpr(SC) asm volatile("s_add_i32 m0, %2, %3\n\ts_nop 0\n\tglobal_load_lds_dwordx4 %0, %1 offset:%4 sc1"::"v"(voff),"s"(sb),"s"(lw),"n"(C-OFF),"n"(OFF):"memory","scc");
  else asm volatile("s_add_i32 m0, %2, %3\n\ts_nop 0\n\tglobal_load_lds_dwordx4 %0, %1 offset:%4"::"v"(voff),"s"(sb),"s"(lw),"n"(C-OFF),"n"(OFF):"memory","scc"); }
template<int L> DI unsigned addlane(unsigned tab,unsigned v){ unsigned r,t; asm volatile("v_readlane_b32 %1, %2, %4\n\ts_nop 1\n\tv_add_u32 %0, %1, %3":"=v"(r),"=&s"(t):"v"(tab),"v"(v),"n"(L)); return r; }
DI void glds4(const char* sb, unsigned voff, unsigned ldsaddr){
  asm volatile("s_mov_b32 m0, %2\n\ts_nop 0\n\tglobal_load_lds_dword %0, %1"::"v"(voff),"s"(sb),"s"(ldsaddr):"memory"); }
DI void glds16q(const char* sb, unsigned voff, unsigned ldsaddr){
  asm volatile("s_mov_b32 m0, %2\n\ts_mov_b64 exec, 0xffff\n\tglobal_load_lds_dwordx4 %0, %1\n\ts_mov_b64 exec, -1"::"v"(voff),"s"(sb),"s"(ldsaddr):"memory"); }
DI void glds16v(const char* vp, unsigned ldsaddr){
  asm volatile("s_mov_b32 m0, %1\n\ts_nop 0\n\tglobal_load_lds_dwordx4 %0, off"::"v"(vp),"s"(ldsaddr):"memory"); }
DI void gld(u32x4& a,unsigned voff,const char* sb){ asm volatile("global_load_dwordx4 %0, %1, %2":"=&v"(a):"v"(voff),"s"(sb):"memory"); }
DI void gldc(u32x4& a,unsigned voff,const char* sb){ asm volatile("global_load_dwordx4 %0, %1, %2 sc1":"=&v"(a):"v"(voff),"s"(sb):"memory"); }
DI void gstc(const u32x4& a,unsigned voff,char* sb){ asm volatile("global_store_dwordx4 %1, %0, %2 sc1\n\ts_nop 1"::"v"(a),"v"(voff),"s"(sb):"memory"); }
DI void gsto(const u32x4& a,unsigned voff,char* sb){ asm volatile("global_store_dwordx4 %1, %0, %2\n\ts_nop 1"::"v"(a),"v"(voff),"s"(sb):"memory"); }
DI void gstc2(const u32x2& a,unsigned voff,char* sb){ asm volatile("global_store_dwordx2 %1, %0, %2 sc1"::"v"(a),"v"(voff),"s"(sb):"memory"); }
DI void gstc1(unsigned a,unsigned voff,char* sb){ asm volatile("global_store_dword %1, %0, %2 sc1"::"v"(a),"v"(voff),"s"(sb):"memory"); }
DI void gld1(unsigned& a,unsigned voff,const char* sb){ asm volatile("global_load_dword %0, %1, %2":"=&v"(a):"v"(voff),"s"(sb):"memory"); }
DI void gld1c(unsigned& a,unsigned voff,const char* sb){ asm volatile("global_load_dword %0, %1, %2 sc1":"=&v"(a):"v"(voff),"s"(sb):"memory"); }
DI void gld2c(u32x2& a,unsigned voff,const char* sb){ asm volatile("global_load_dwordx2 %0, %1, %2 sc1":"=&v"(a):"v"(voff),"s"(sb):"memory"); }
template<int ST> DI void gld4m(unsigned* a,unsigned voff,const char* sb,unsigned m){
  asm volatile("s_cmp_eq_u32 %6, 0\n\ts_cbranch_scc1 .Lskip%=\n\tglobal_load_dword %0, %4, %5 sc1\n\tglobal_load_dword %1, %4, %5 offset:%7 sc1\n\tglobal_load_dword %2, %4, %5 offset:%8 sc1\n\tglobal_load_dword %3, %4, %5 offset:%9 sc1\n.Lskip%=:"
    :"=&v"(a[0]),"=&v"(a[1]),"=&v"(a[2]),"=&v"(a[3]):"v"(voff),"s"(sb),"s"(m),"n"(ST),"n"(2*ST),"n"(3*ST):"memory","scc"); }
template<int N> DI void wait_vm4m(unsigned* a,unsigned m){
  asm volatile("s_cmp_eq_u32 %4, 0\n\ts_cbranch_scc1 .Lskip%=\n\ts_waitcnt vmcnt(%5)\n.Lskip%=:":"+v"(a[0]),"+v"(a[1]),"+v"(a[2]),"+v"(a[3]):"s"(m),"n"(N):"memory","scc"); }
DI void gst4m(const u32x2& a,const u32x2& b,const u32x2& c,const u32x2& d,unsigned voff,char* sb,unsigned m){
  asm volatile("s_cmp_eq_u32 %6, 0\n\ts_cbranch_scc1 .Lskip%=\n\tglobal_store_dwordx2 %4, %0, %5 sc1\n\tglobal_store_dwordx2 %4, %1, %5 offset:512 sc1\n\tglobal_store_dwordx2 %4, %2, %5 offset:1024 sc1\n\tglobal_store_dwordx2 %4, %3, %5 offset:1536 sc1\n.Lskip%=:"
    ::"v"(a),"v"(b),"v"(c),"v"(d),"v"(voff),"s"(sb),"s"(m):"memory","scc"); }
template<class T> DI void pin(T& a){ asm volatile("":"+v"(a)); }
template<int N> DI void wait_vm4(u32x4& a,u32x4& b,u32x4& c,u32x4& d){ asm volatile("s_waitcnt vmcnt(%4)":"+v"(a),"+v"(b),"+v"(c),"+v"(d):"n"(N):"memory"); }
template<int N> DI void wait_vm3(u32x4& a,u32x4& b,u32x4& c){ asm volatile("s_waitcnt vmcnt(%3)":"+v"(a),"+v"(b),"+v"(c):"n"(N):"memory"); }
template<int N> DI void wait_vm2(u32x4& a,u32x4& b){ asm volatile("s_waitcnt vmcnt(%2)":"+v"(a),"+v"(b):"n"(N):"memory"); }
template<int C> DI float dppf(float v){ return __uint_as_float(__builtin_amdgcn_update_dpp(0u,__float_as_uint(v),C,0xf,0xf,true)); }
DI float sum8(float v){ v+=dppf<0xB1>(v); v+=dppf<0x4E>(v); v+=dppf<0x141>(v); return v; }
DI float xsum32(float v){ const auto r=__builtin_amdgcn_permlane32_swap(__float_as_uint(v),__float_as_uint(v),false,false); return __uint_as_float(r[0])+__uint_as_float(r[1]); }
DI bf16x8 pack8(const f32x16& c,int o){ u32x4 r={pk2(c[o],c[o+1]),pk2(c[o+2],c[o+3]),pk2(c[o+4],c[o+5]),pk2(c[o+6],c[o+7])}; return BF8(r); }

constexpr size_t WS_FLAG=0;
constexpr size_t WS_A=32768, WS_Q=36864;
constexpr int    KVP=5632;
constexpr size_t WS_KVP=65536;
constexpr size_t WS_O=WS_KVP+256*KVP;
constexpr size_t WS_H=WS_O+(size_t)S*D*2;
constexpr size_t WS_ST=WS_H+(size_t)S*D*2;
constexpr size_t WS_CS=WS_ST+9*S*16;
constexpr size_t WS_X=WS_CS+32*3*32*16;
constexpr size_t WS_R=WS_X+64*16*8192;
constexpr size_t WS_G=WS_R+16*256*1024;

DI void wait_progress(char* ws,int who,int n,unsigned base,unsigned want,bool serve=false){
  const int lane=lane_id();
  const GAS unsigned* p=(const GAS unsigned*)(ws+WS_FLAG)+(lane<n?who:0)*32;
  for(unsigned spins=0;spins<(1u<<18);spins++){
    const unsigned v=__hip_atomic_load(p,RLX_AGENT);
    if(serve){ unsigned qv[4]; unsigned long long av[4];
      #pragma unroll
      for(int i=0;i<4;i++){ qv[i]=__hip_atomic_load((const GAS unsigned*)(ws+WS_Q)+i*64+lane,RLX_AGENT); av[i]=__hip_atomic_load((const GAS unsigned long long*)(ws+WS_A)+i*64+lane,RLX_AGENT); }
      #pragma unroll
      for(int i=0;i<4;i++){ const unsigned long long a=((unsigned long long)base<<32)|qv[i]; if(av[i]!=a) __hip_atomic_store((GAS unsigned long long*)(ws+WS_A)+i*64+lane,a,RLX_AGENT); } }
    if(__all(lane>=n || v-base-want<8u-want)) break;
    __builtin_amdgcn_s_sleep(1);
  }
}
DI void meet4(LAS unsigned* c){
  if(lane_id()==0) __hip_atomic_fetch_add(c,1u,__ATOMIC_RELAXED,__HIP_MEMORY_SCOPE_WORKGROUP);
  while(__builtin_amdgcn_readfirstlane(__hip_atomic_load(c,__ATOMIC_RELAXED,__HIP_MEMORY_SCOPE_WORKGROUP))<4u) __builtin_amdgcn_s_sleep(1);
  asm volatile("":::"memory"); }
DI void publish(char* ws,int bx,unsigned v){ __hip_atomic_store((GAS unsigned*)(ws+WS_FLAG)+bx*32,v,RLX_AGENT); }

constexpr int slot4(int p){ return (p&3)==3?114688:8192+(p&3)*32768; }
constexpr int ex4(int p){ return (p<=12&&(p&3)==0)||p==13; }
constexpr int st0(int p){ return 8*p+4*(p>13?5:(p+3)>>2); }
constexpr int stg(int u){ int p=0; while(p<17&&st0(p+1)<=u) p++; return p; }

__global__ __launch_bounds__(512,2) void mega(const char* __restrict__ x,const char* __restrict__ wqkv,const char* __restrict__ wphi,const char* __restrict__ wo,
                                              const char* __restrict__ wg,const char* __restrict__ wu,const char* __restrict__ wd,char* __restrict__ ws,char* __restrict__ outp){
  extern __shared__ __attribute__((aligned(16))) char lds_[];
  LAS char* lds=(LAS char*)lds_; const unsigned lb=(unsigned)(size_t)lds;
  const int w=__builtin_amdgcn_readfirstlane(threadIdx.x>>6);
  const int bx=blockIdx.x, xcd=bx&7, ii=bx>>3;
  const char* pc0; asm volatile("s_getpc_b64 %0":"=s"(pc0));
  unsigned base;
  {
    const int lane=lane_id(), hh=lane>>5, r=lane&31; const unsigned l16=lane*16;
    const int hg=4*(xcd&1)+(ii&3), tk=8*(xcd>>1)+(ii>>2), tg=w&1, hp=(w>>1)&1;
    constexpr int RB=73728, SL=5120, TI=114688, KVI=156672, RS=162832, QC=163824, BW=163832, PD=2;
    const int q=(lane&15)>>2, p=lane&3, g2=(lane>>4)&1, sg=2*g2+(p>>1), kw=w&3;
    const int rot=((ii>>2)+2*(ii&3))%9;
    auto ub=[&](int u){ const int v=u+rot; return wqkv+hg*144+(size_t)(v>=9?v-9:v)*(64*3456); };
    const unsigned ab=lb+RB+(8*hh+q)*320+72*hp+32*(p&1), aB_=lb+tg*36864+hh*512;
    const int tl=tg*32+r, sig=(((tl>>1)&1)<<2)|(tl&1)|(((tl>>2)&1)<<1);
    LAS char* trow=lds+TI+((2*hp+hh)*64+tl)*128;
    unsigned vk[5];
    auto mkvk=[&](){
      #pragma unroll
      for(int i=0;i<5;i++){ const unsigned c=i*64+lane, row=(c*205u)>>12, cc=c-row*20, t=cc>=10, c2=cc-10*t; vk[i]=(kw*16+row)*3456+1152*(1+t)+(c2<8?c2:8)*16; } };
    auto dma=[&](int u){
      #pragma unroll
      for(int i=0;i<5;i++) glds16(ub(u),vk[i],lb+RB+(4*(u&3)+kw)*SL+i*1024); };
    u32x4 pw[6];
    constexpr int PHS=1440;
    auto ldpw=[&](){ const unsigned b1=lb+KVI+kw*PHS+(hh*16+q)*40+32, b0=b1-32+8*sg;
      const unsigned mp=(((r>>2)&1)==hh)?~0u:0u, m0=hh?0u:mp, m1=hh?mp:0u;
      #pragma unroll
      for(int n=0;n<6;n++){ const unsigned b=(n<3?b0:b1)+(n%3)*320;
        const u32x2 lo=__builtin_bit_cast(u32x2,trd((LAS char*)(size_t)b));
        if(n%3<2){ const u32x2 hi=__builtin_bit_cast(u32x2,trd((LAS char*)(size_t)(b+160))); pw[n]=u32x4{lo[0]&(n%3?mp:m0),lo[1]&mp,hi[0]&mp,hi[1]&mp}; }
        else pw[n]=u32x4{lo[0]&mp,lo[1]&m1,0u,0u}; } };
    auto feat=[&](const f32x16& c0,const f32x16& c1,float rs,float* e1,float* e2,float& s1,float& s2){
      f32x16 p0={},p1={};
      { const bf16x8 b0=pack8(c0,0), b1=pack8(c0,8), b2=pack8(c1,0);
        p0=MFMA32(BF8(pw[0]),b0,p0); p1=MFMA32(BF8(pw[3]),b0,p1);
        p0=MFMA32(BF8(pw[1]),b1,p0); p1=MFMA32(BF8(pw[4]),b1,p1);
        p0=MFMA32(BF8(pw[2]),b2,p0); p1=MFMA32(BF8(pw[5]),b2,p1); }
      float pv[18];
      #pragma unroll
      for(int i=0;i<16;i++) pv[i]=p0[i];
      pv[16]=p1[0]; pv[17]=p1[1];
      float mx=pv[0],mn=pv[0];
      #pragma unroll
      for(int i=1;i<18;i++){ mx=fmaxf(mx,pv[i]); mn=fminf(mn,pv[i]); }
      const float L=1.44269504f*rs, a=-mx*L, b=mn*L; s1=0.f; s2=0.f;
      #pragma unroll
      for(int i=0;i<18;i++){ e1[i]=__builtin_amdgcn_exp2f(fmaf(pv[i],L,a)); e2[i]=__builtin_amdgcn_exp2f(fmaf(pv[i],-L,b)); s1+=e1[i]; s2+=e2[i]; } };
    char* gd=ws+WS_KVP+(size_t)(hg*32+tk)*KVP+kw*1408; char* dst=ws+WS_O+(size_t)(tk*2+tg)*36*1024; const int pr=2*hg+hp;
    u32x2 av; u32x4 xv[18]; unsigned cp;
    if(w<4){
      __builtin_amdgcn_s_setprio(3);
      gld2c(av,0u,ws+WS_A+bx*8);
      const char* xs=x+(size_t)(tk*64+w*16)*1152; const unsigned xo=(lane>>3)*1152+(lane&7)*16;
      #pragma unroll
      for(int i=0;i<18;i++){ const int c=i%9+rot; gld(xv[i],xo,xs+(c>=9?c-9:c)*128+(i/9)*9216); }
      __builtin_amdgcn_s_setprio(0);
    } else {
      { const unsigned o=(ii*4+kw)*8192+lane*128; gld1(cp,o<CODE_PF?o:0u,pc0); }
      mkvk(); dma(0); dma(1);
    }
    int w2=w; asm volatile("":"+s"(w2));
    unsigned aB[4];
    #pragma unroll
    for(int i=0;i<4;i++) aB[i]=aB_+(r^(2*i+hh))*16;
    if(w2<4){
      const unsigned mk=__builtin_amdgcn_readfirstlane((bx==0&&w==0)?~0u:0u);
      unsigned pg[4], qv[4], nb=0;
      gld4m<128>(pg,lane*512,ws+WS_FLAG,mk);
      mkvk();
      unsigned vq[3];
      { unsigned c=w*64+lane, row=c/9; vq[0]=row*3456+(c-row*9)*16; c+=256; row=c/9; vq[1]=row*3456+(c-row*9)*16; c=512+16*w+(lane&15); row=c/9; vq[2]=row*3456+(c-row*9)*16; }
      auto ldq=[&](int kg,int gp){ const char* s=ub(kg); const unsigned L=lb+RB+gp*9216;
        glds16(s,vq[0],L+w*1024); glds16(s,vq[1],L+4096+w*1024); glds16q(s,vq[2],L+8192+w*256); };
      wait_vm<18>(); pin(av);
      const unsigned req=av[0]+1u;
      if(w==0&&lane==0) gstc1(req,0u,ws+WS_Q+bx*4);
      auto var=[&](int h){ f32x4 ga={},gb={};
        #pragma unroll
        for(int i=0;i<9;i++){ const s16x4 lo=__builtin_bit_cast(s16x4,(u32x2{xv[9*h+i][0],xv[9*h+i][1]})), hi=__builtin_bit_cast(s16x4,(u32x2{xv[9*h+i][2],xv[9*h+i][3]}));
          ga=__builtin_amdgcn_mfma_f32_4x4x4bf16_1k(lo,lo,ga,0,0,0); gb=__builtin_amdgcn_mfma_f32_4x4x4bf16_1k(hi,hi,gb,0,0,0); }
        ga+=gb;
        const float qq=sum8((lane&2)?((lane&1)?ga[3]:ga[2]):((lane&1)?ga[1]:ga[0]));
        if((lane&7)==0) *(LAS float*)(lds+RS+(w*16+8*h+(lane>>3))*4)=__builtin_amdgcn_rsqf(qq*(1.f/D)+1e-5f); };
      { const bool dg=(lane>>4)==((lane&15)>>2); const unsigned o=dg?((lane&1)?0x3f800000u:0x00003f80u):0u, o0=(lane&2)?0u:o, o1=(lane&2)?o:0u, on=dg?0x3f803f80u:0u;
        const bf16x8 sa=BF8((u32x4{o0,o1,0u,0u})), sb=BF8((u32x4{0u,0u,o0,o1})), sq=BF8((u32x4{on,on,on,on}));
        static_for<0,2>([&](auto H){ constexpr int h=decltype(H)::value;
          f32x4 sm={};
          static_for<0,3>([&](auto G){ constexpr int g=9*h+3*decltype(G)::value; wait_vm3<15-g>(xv[g],xv[g+1],xv[g+2]);
            sm=MFMA16(sq,BF8(xv[g]),sm); sm=MFMA16(sq,BF8(xv[g+1]),sm); sm=MFMA16(sq,BF8(xv[g+2]),sm); SB(); });
          const float m=sum8(sm[0])*(-1.f/D); const f32x4 nm={m,m,m,m};
          const int t5=(w&1)*16+8*h+(lane>>3), sub=lane&7;
          LAS char* xd=lds+((w>>1)*36+(sub>>1))*1024+((sub&1)*32+(t5^sub))*16;
          #pragma unroll
          for(int i=0;i<9;i++){ const f32x4 a=MFMA16(sa,BF8(xv[9*h+i]),nm), b=MFMA16(sb,BF8(xv[9*h+i]),nm);
            xv[9*h+i]=u32x4{pk2(a[0],a[1]),pk2(a[2],a[3]),pk2(b[0],b[1]),pk2(b[2],b[3])};
            *(LAS u32x4*)(xd+i*4096)=xv[9*h+i]; }
          if constexpr(h==0) var(0);
          SB(); }); }
      wait_lgkm0(); bar();
      auto ask=[&](){ gld4m<256>(qv,lane*4,ws+WS_Q,mk); };
      auto echo=[&](){ gst4m(u32x2{qv[0],nb},u32x2{qv[1],nb},u32x2{qv[2],nb},u32x2{qv[3],nb},lane*8,ws+WS_A,mk); };
      pin(pg[0]); pin(pg[1]); pin(pg[2]); pin(pg[3]);
      if(mk){ nb=(__builtin_amdgcn_readfirstlane(pg[0])+8u)&~7u;
        while(__any((pg[0]^nb)<8u||(pg[1]^nb)<8u||(pg[2]^nb)<8u||(pg[3]^nb)<8u)) nb+=8u; }
      ask();
      bar();
      dma(4); var(1);
      bar();
      dma(5);
      bar();
      dma(6); wait_vm<10>(); wait_vm4m<10>(qv,mk); echo(); ask();
      bar();
      dma(7); wait_vm<10>();
      bar();
      dma(8); wait_vm<10>();
      unsigned ct[2], cp2=0, cq2=0;
      {
        const int c=w*81+lane, c2=c+(lane<17?64:0); const char* m=((xcd&1)?wu:wg)+(size_t)ii*18*(F*2);
        auto off=[&](int e){ const int row=e/18, l=e-row*18; return (unsigned)(row*(F*2)+(l<16?(xcd>>1)*2048+l*128:8192+(xcd>>1)*256+(l-16)*128)); };
        gld1(ct[0],off(c),m); gld1(ct[1],off(c2),m); }
      bar();
      ldq(0,3); wait_vm<10>();
      bar();
      wait_vm<5>(); wait_lgkm0();
      bar();
      wait_vm4m<5>(qv,mk); echo();
      ldq(1,8);
      if(w<3){ if(ii<18){ const int u=(xcd&1)*18+ii; gld1(cp2,((u&3)*144+w*48+(lane<48?lane:0))*1152,wo+(u>>2)*128); }
        if(w<2) gld1(cq2,((ii&3)*48+(lane<48?lane:0))*(F*2),(w?wu:wg)+(8*(xcd>>1)+(ii>>2))*256); }
      const float rs=*(LAS float*)(lds+RS+tl*4);
      ldpw();
      if(w==0&&lane==0) *(LAS u32x2*)(lds+QC)=u32x2{0u,0u};
      wait_lgkm0(); bar();
      gld2c(av,0u,ws+WS_A+bx*8);
      {
        f32x16 kb={}; bf16x8 a[4],b[4];
        #pragma unroll
        for(int s=0;s<4;s++){ s16x4 ta[2],tb[2];
          #pragma unroll
          for(int t=0;t<2;t++){ const int sx=((q>>1)<<2)|(q&1)|(t<<1);
            LAS char* rp=lds+TI+(kw*64+16*s+8*hh+4*t+q)*128+(p&1)*8;
            ta[t]=trd(rp+(((4+sg)^sx)*16)); tb[t]=trd(rp+((((5+sg)&7)^sx)*16)); }
          a[s]=cat(ta[0],ta[1]); b[s]=cat(tb[0],tb[1]); }
        SB();
        #pragma unroll
        for(int s=0;s<4;s++) kb=MFMA32(a[s],b[s],kb);
        const int rho=r-2*(kw&1), row=(r>=20)?r-2:rho;
        if(hh==0 && (r==20 || r==21 || (rho>=0&&rho<18))) gstc2(r==21?u32x2{0u,0u}:u32x2{pk2(kb[0],kb[1]),pk2(kb[2],kb[3])},row*8,gd+1216); }
      ldq(2,0); ldq(3,1); ldq(4,2);
      wait_vm<9>(); pin(av); pin(ct[0]); pin(ct[1]); pin(cp2); pin(cq2);
      if(w==0){
        if(bx!=0){ unsigned long long a=((unsigned long long)av[1]<<32)|av[0];
          for(unsigned spins=0;(unsigned)a!=req&&spins<(1u<<18);spins++){ __builtin_amdgcn_s_sleep(1); a=__hip_atomic_load((const GAS unsigned long long*)(ws+WS_A)+bx,RLX_AGENT); }
          nb=(unsigned)(a>>32); }
        *(LAS unsigned*)(lds+BW)=nb; }
      wait_lgkm0(); bar();
      const unsigned aQ0=lb+RB+(8*hh+q)*144+72*hp+32*(p&1)+8*sg, aQ1=aQ0-8*sg+32, aQ0h=aQ0+36864, aQ1h=aQ1+36864;
      f32x16 c0={},c1={}; bf16x8 fa[PD+1],fb[PD+1],fc[PD+1];
      static_for<0,36+PD>([&](auto SS){ constexpr int s=decltype(SS)::value, m=s-PD, kg=s>>2, gp=(kg==0)?3:(kg==1)?8:(kg<5)?kg-2:kg-1, o=(gp&3)*9216+(gp==8?36864:0)+(s&3)*2304;
        if constexpr(s==8){ wait_vm<12>(); meet4((LAS unsigned*)(lds+QC)); }
        if constexpr(s==20){ wait_vm<0>(); meet4((LAS unsigned*)(lds+QC+4)); }
        if constexpr(s<36){ fa[s%(PD+1)]=ldtr<576>(((gp>=4)?aQ0h:aQ0)+o); fc[s%(PD+1)]=ldtr<576>(((gp>=4)?aQ1h:aQ1)+o); fb[s%(PD+1)]=*(LAS bf16x8*)(size_t)(aB[s&3]+s*1024); SB(); }
        if constexpr(m>=0){ c0=MFMA32(fa[m%(PD+1)],fb[m%(PD+1)],c0); c1=MFMA32(fc[m%(PD+1)],fb[m%(PD+1)],c1); SB(); }
        if constexpr(s>=3&&s<7){ ldq(s+2,s+1); SB(); }
      });
      base=__builtin_amdgcn_readfirstlane(*(LAS unsigned*)(lds+BW));
      unsigned ph[20]; float s1,s2;
      { float e1[18],e2[18]; feat(c0,c1,rs,e1,e2,s1,s2);
        const float a2=s1*__builtin_amdgcn_rcpf(s2);
        #pragma unroll
        for(int i=0;i<9;i++){ ph[i]=pk2(e1[2*i],e1[2*i+1]); ph[9+i]=pk2(e2[2*i]*a2,e2[2*i+1]*a2); }
        ph[18]=0u; ph[19]=0u; }
      const bool mine=((r>>2)&1)==hh;
      const LAS char* k0=lds+KVI+(mine?((2*hp+hh)*19+4*(r>>3)+(r&3))*80:6080);
      const LAS char* k1=lds+KVI+((mine&&r<8&&(r&3)<3)?((2*hp+hh)*19+16+(r&3))*80:6080);
      bar();
      f32x16 o0={},o1={};
      #pragma unroll
      for(int kp=0;kp<5;kp++){ const bf16x8 b=BF8((u32x4{ph[4*kp],ph[4*kp+1],ph[4*kp+2],ph[4*kp+3]}));
        o0=MFMA32(*(const LAS bf16x8*)(k0+kp*16),b,o0); o1=MFMA32(*(const LAS bf16x8*)(k1+kp*16),b,o1); }
      const float id=__builtin_amdgcn_rcpf(fmaf(s1,1e-6f,o1[2]));
      gstc(u32x4{pk2(o0[0]*id,o0[1]*id),pk2(o0[2]*id,o0[3]*id),pk2(o0[4]*id,o0[5]*id),pk2(o0[6]*id,o0[7]*id)},l16,dst+(2*pr)*1024);
      gstc(u32x4{pk2(o0[8]*id,o0[9]*id),pk2(o0[10]*id,o0[11]*id),pk2(o0[12]*id,o0[13]*id),pk2(o0[14]*id,o0[15]*id)},l16,dst+(2*pr+1)*1024);
      { const unsigned t=pk2(o1[0]*id,o1[1]*id); const auto sw=__builtin_amdgcn_permlane32_swap(t,t,false,false);
        if(hh==0) gstc2(u32x2{sw[0],sw[1]},l16,dst+(32+(pr>>2))*1024+((pr>>1)&1)*512+(pr&1)*8); }
      wait_vm<0>();
    } else {
      { const char* ps=wphi+(2*hg+hp)*1296;
        #pragma unroll
        for(int i=0;i<6;i++){ const unsigned c=(i<5?i*64:296)+lane, row=(c*205u)>>11, cc=c-row*10; glds4(ps,row*36+(cc<9?cc:8)*4,lb+KVI+kw*PHS+(i<5?i*256:1184)); }
      }
      dma(2); dma(3);
      unsigned kd[3];
      #pragma unroll
      for(int c=0;c<3;c++){ const int g=2*(32*c+(lane&15)+16*(lane>>5))+((lane>>4)&1), ty=g>=76, sl=(g-76*ty)>>1;
        kd[c]=g<171?lb+KVI+kw*1520+(g<152?(sl>>1)*80+(4*ty+2*(g&1)+(sl&1))*8:(g-152)*80+64):0u; }
      u32x4 sel; { const unsigned o=(((lane>>3)^(lane>>4))&1)?0u:((lane&1)?0x3f800000u:0x00003f80u); const int j=(lane&7)>>1; sel=u32x4{j==0?o:0u,j==1?o:0u,j==2?o:0u,j==3?o:0u}; }
      pin(kd[0]); pin(kd[1]); pin(kd[2]); pin(sel);
      wait_vm<21>(); pin(cp);
      const unsigned aA0=ab+8*sg, aA1=ab+(sg==0?32:160+8*sg), aA2=ab+160+(sg==0?0:32), aA0h=aA0+8*SL, aA1h=aA1+8*SL, aA2h=aA2+8*SL;
      bar();
      f32x16 c0={},c1={},c2={}; bf16x8 fa0[PD+1],fa1[PD+1],fa2[PD+1],fb[PD+1];
      static_for<0,36+PD>([&](auto SS){ constexpr int s=decltype(SS)::value, m=s-PD;
        if constexpr(s>0&&s<36&&(s&3)==0){
          if constexpr(s==4) wait_vm<10>();
          if constexpr(s==8) wait_vm<5>();
          if constexpr(s==12) wait_vm<0>();
          if constexpr(s!=28) wait_lgkm0();
          bar(); }
        if constexpr(s<36){ fb[s%(PD+1)]=*(LAS bf16x8*)(size_t)(aB[s&3]+s*1024); fa0[s%(PD+1)]=ldtr<1280>(((s&8)?aA0h:aA0)+(s&7)*SL); SB(); }
        if constexpr(m>=0){ c0=MFMA32(fa0[m%(PD+1)],fb[m%(PD+1)],c0); SB(); }
        if constexpr(s<36){ fa1[s%(PD+1)]=ldtr<1280>(((s&8)?aA1h:aA1)+(s&7)*SL); SB(); }
        if constexpr(m>=0){ c1=MFMA32(fa1[m%(PD+1)],fb[m%(PD+1)],c1); SB(); }
        if constexpr(s<36){ fa2[s%(PD+1)]=ldtr<1280>(((s&8)?aA2h:aA2)+(s&7)*SL); SB(); }
        if constexpr(m>=0){ c2=MFMA32(fa2[m%(PD+1)],fb[m%(PD+1)],c2); SB(); }
        if constexpr(s==7){ ldpw(); SB(); }
      });
      { const float rs=*(LAS float*)(lds+RS+tl*4);
        float e1[18],e2[18],s1,s2; feat(c0,c1,rs,e1,e2,s1,s2);
        *(LAS u32x4*)(trow+((5^sig)*16))=u32x4{pk2(c2[0],c2[1]),pk2(c2[2],c2[3]),pk2(c1[4],c1[5]),pk2(c1[6],c1[7])};
        *(LAS u32x4*)(trow+((7^sig)*16))=u32x4{pk2(c2[4],c2[5]),pk2(c2[6],c2[7]),pk2(__builtin_amdgcn_rcpf(rs),0.f),0u};
        *(LAS u32x4*)(trow+((6^sig)*16))=__builtin_bit_cast(u32x4,pack8(c1,8));
        const float a1=rs*__builtin_amdgcn_rcpf(s1), a2=rs*__builtin_amdgcn_rcpf(s2); unsigned ph[20];
        #pragma unroll
        for(int i=0;i<9;i++){ ph[i]=pk2(e1[2*i]*a1,e1[2*i+1]*a1); ph[9+i]=pk2(e2[2*i]*a2,e2[2*i+1]*a2); }
        ph[18]=0u; ph[19]=0u;
        #pragma unroll
        for(int i=0;i<5;i++) *(LAS u32x4*)(trow+((i^sig)*16))=u32x4{ph[4*i],ph[4*i+1],ph[4*i+2],ph[4*i+3]}; }
      wait_lgkm0(); bar();
      const int rt=ii&28, tq=(r+rt)&31, who=((tq&7)*4+(hg&3))*8+(tq>>3)*2+(hg>>2);
      {
        f32x16 ka={}; bf16x8 a[4],b[4];
        #pragma unroll
        for(int s=0;s<4;s++){ s16x4 ta[2],tb[2];
          #pragma unroll
          for(int t=0;t<2;t++){ const int sx=((q>>1)<<2)|(q&1)|(t<<1);
            LAS char* rp=lds+TI+(kw*64+16*s+8*hh+4*t+q)*128+(p&1)*8;
            ta[t]=trd(rp+((sg^sx)*16)); tb[t]=trd(rp+((((5+sg)&7)^sx)*16)); }
          a[s]=cat(ta[0],ta[1]); b[s]=cat(tb[0],tb[1]); }
        { LAS u32x4* z=(LAS u32x4*)(lds+KVI)+kw*95+lane; z[0]=u32x4{0u,0u,0u,0u}; if(lane<(kw==3?36:31)) z[64]=u32x4{0u,0u,0u,0u}; }
        SB();
        #pragma unroll
        for(int s=0;s<4;s++)ka=MFMA32(a[s],b[s],ka);
        const int rho=r-2*(kw&1), row=(r>=20)?r-2:rho;
        if(r==20 || (rho>=0&&rho<18)){ gstc(__builtin_bit_cast(u32x4,pack8(ka,0)),row*32+hh*16,gd); gstc(__builtin_bit_cast(u32x4,pack8(ka,8)),row*32+hh*16,gd+608); }
        }
      wait_vm<0>();
      bar();
      base=__builtin_amdgcn_readfirstlane(*(LAS unsigned*)(lds+BW));
      if(w==7&&lane==0) publish(ws,bx,base+1);
      {
        const char* pb=ws+WS_KVP+(size_t)hg*32*KVP+kw*1408;
        const unsigned g0=r*16+hh*KVP, g2_=(r<22?r:21)*16+hh*KVP+1024;
        constexpr int NW=10; u32x4 pv[NW][3]; f32x4 ac[3]={};
        auto use=[&](auto I,auto N){ constexpr int i=decltype(I)::value%NW; wait_vm3<decltype(N)::value>(pv[i][0],pv[i][1],pv[i][2]);
          ac[0]=MFMA16(BF8(sel),BF8(pv[i][0]),ac[0]); ac[1]=MFMA16(BF8(sel),BF8(pv[i][1]),ac[1]); ac[2]=MFMA16(BF8(sel),BF8(pv[i][2]),ac[2]); SB(); };
        if(bx==0&&w==7) wait_progress(ws,who,32,base,1,true);
        unsigned rdy=0;
        static_for<0,8>([&](auto G){ constexpr int g=decltype(G)::value;
          for(unsigned spins=0;(~rdy&(unsigned)((16ull<<(4*g))-1))!=0u&&spins<(1u<<18);spins++){
            if(spins) __builtin_amdgcn_s_sleep(2);
            rdy=(unsigned)__ballot(__hip_atomic_load((const GAS unsigned*)(ws+WS_FLAG)+who*32,RLX_AGENT)-base-1u<7u); }
          static_for<2*g,2*g+2>([&](auto I){ constexpr int i=decltype(I)::value;
            if constexpr(i>=NW) use(IC(i-NW),IC(3*NW-3));
            const char* s=pb+(size_t)((2*i+rt)&31)*KVP; gldc(pv[i%NW][0],g0,s); gldc(pv[i%NW][1],g0,s+512); gldc(pv[i%NW][2],g2_,s); SB(); }); });
        static_for<16-NW,16>([&](auto I){ use(I,IC(3*(15-decltype(I)::value))); });
        #pragma unroll
        for(int c=0;c<3;c++) if(kd[c]) *(LAS u32x2*)(size_t)kd[c]=u32x2{pk2(ac[c][0],ac[c][1]),pk2(ac[c][2],ac[c][3])}; }
      wait_lgkm0(); bar();
      if(ii<18){
        const int np=((xcd&1)*18+ii)>>2; const unsigned co=((lane&7)^(((lane>>4)&1)<<2))*16;
        #pragma unroll
        for(int v=0;v<2;v++){ const int rl=8*(kw+4*v)+(lane>>3), hk=(rl>>3)&1, j=rl&7;
          #pragma unroll
          for(int kb=0;kb<9;kb++){ const int s0=kb*4+((kw+4*v)>>1), h2=s0>=18, s1=s0-18*h2+2*np, s=18*h2+(s1>=18?s1-18:s1);
            const int row=(s<32)?36*(s>>1)+8*(s&1)+18*hk+j:144*(s-32)+16+72*hk+36*(j>>2)+18*((j>>1)&1)+(j&1);
            glds16(wo+np*128,row*1152+co,lb+RB+kb*8192+(kw+4*v)*1024); } } }
    }
    bar();
    if(w==0 && lane==0) publish(ws,bx,base+2);
  }
  const char* gA=wg+(8*(xcd>>1)+(ii>>2))*256; const char* uA=wu+(8*(xcd>>1)+(ii>>2))*256;
  asm volatile("":"+s"(gA),"+s"(uA));
  auto p3pre=[&](){
    const int lane=lane_id();
    const unsigned voffS=(unsigned)((8*w+(lane>>3))*(F*2)+(((lane&7)^(((lane>>4)&1)<<2))*16));
    #pragma unroll
    for(int kb=0;kb<3;kb++){ glds16(gA+(size_t)kb*64*F*2,voffS,lb+kb*16384+w*1024); glds16(uA+(size_t)kb*64*F*2,voffS,lb+kb*16384+8192+w*1024); } };
  if(ii<18){
    const int lane=lane_id(), hh=lane>>5, r=lane&31; const unsigned l16=lane*16;
    const int u=(xcd&1)*18+ii, np=u>>2, tk=4*(xcd>>1)+(u&3), tg=w&3, kh=w>>2;
    if(w==0) wait_progress(ws,(((2*tk+((lane>>3)&1))&7)*4+(lane&3))*8+(xcd&6)+((lane>>2)&1),16,base,2,bx==0);
    wait_vm<0>(); bar();
    u32x4 ov[18], xr[2];
    const char* os=ws+WS_O+((size_t)(tk*4+tg)*36+kh*18)*1024;
    auto ldo=[&](int i){ const int s=i+2*np; gldc(ov[i],l16,os+(s>=18?s-18:s)*1024); };
    {
      #pragma unroll
      for(int i=0;i<8;i++) ldo(i); }
    const int q=(lane&15)>>2, p=lane&3, g2=(lane>>4)&1;
    const unsigned a0_=(8*hh+q)*128+(((2*g2+(p>>1))^((q>>1)<<2))*16)+(p&1)*8, a0=lb+73728+kh*18*2048+a0_, a1=lb+73728+kh*18*2048+(a0_^64);
    f32x16 c0={},c1={};
    static_for<0,9>([&](auto G){ constexpr int g=decltype(G)::value;
      const bf16x8 f0=ldtr<512>(a0+g*4096), f1=ldtr<512>(a1+g*4096), f2=ldtr<512>(a0+g*4096+2048), f3=ldtr<512>(a1+g*4096+2048);
      wait_vm2<(g<7?6:16+4-2*g-2)>(ov[2*g],ov[2*g+1]);
      c0=MFMA32(f0,BF8(ov[2*g]),c0); c1=MFMA32(f1,BF8(ov[2*g]),c1); SB();
      if constexpr(g<5){ ldo(2*g+8); ldo(2*g+9); }
      if constexpr(g==5){ const char* xs=x+((size_t)(tk*128+tg*32)*576+np*64+kh*32)*2; gld(xr[0],r*1152+hh*16,xs); gld(xr[1],r*1152+hh*16,xs+32); }
      c0=MFMA32(f2,BF8(ov[2*g+1]),c0); c1=MFMA32(f3,BF8(ov[2*g+1]),c1); SB(); });
    {
      LAS char* sx=lds+(w<6?49152+w*4096:147456+(w-6)*4096)+l16; LAS char* rx=lds+((w^4)<6?49152+(w^4)*4096:147456+((w^4)-6)*4096)+l16;
      #pragma unroll
      for(int i=0;i<4;i++) *(LAS u32x4*)(sx+i*1024)=kh?u32x4{__float_as_uint(c0[4*i]),__float_as_uint(c0[4*i+1]),__float_as_uint(c0[4*i+2]),__float_as_uint(c0[4*i+3])}
                                                       :u32x4{__float_as_uint(c1[4*i]),__float_as_uint(c1[4*i+1]),__float_as_uint(c1[4*i+2]),__float_as_uint(c1[4*i+3])};
      wait_lgkm0(); bar();
      #pragma unroll
      for(int i=0;i<4;i++){ const u32x4 v=*(LAS u32x4*)(rx+i*1024);
        #pragma unroll
        for(int e=0;e<4;e++) c0[4*i+e]=(kh?c1[4*i+e]:c0[4*i+e])+__uint_as_float(v[e]); } }
    wait_vm2<0>(xr[0],xr[1]);
    unsigned o[8]; float v[16], sm=0.f;
    #pragma unroll
    for(int i=0;i<2;i++){ const auto x0=__builtin_amdgcn_permlane32_swap(xr[i][0],xr[i][2],false,false), x1=__builtin_amdgcn_permlane32_swap(xr[i][1],xr[i][3],false,false);
      #pragma unroll
      for(int e=0;e<2;e++){ const int n=4*(2*i+e);
        o[4*i+2*e]=pk2(c0[n]+lo16(x0[e]),c0[n+1]+hi16(x0[e])); o[4*i+2*e+1]=pk2(c0[n+2]+lo16(x1[e]),c0[n+3]+hi16(x1[e])); } }
    char* dst=ws+WS_H+((size_t)(tk*4+tg)*36+np*4+kh*2)*1024;
    gstc(u32x4{o[0],o[1],o[2],o[3]},l16,dst); gstc(u32x4{o[4],o[5],o[6],o[7]},l16,dst+1024);
    #pragma unroll
    for(int i=0;i<8;i++){ v[2*i]=lo16(o[i]); v[2*i+1]=hi16(o[i]); sm+=v[2*i]+v[2*i+1]; }
    sm=xsum32(sm)*(1.f/32);
    float qq=0.f;
    #pragma unroll
    for(int i=0;i<16;i++){ const float d=v[i]-sm; qq+=d*d; }
    qq=xsum32(qq);
    if(hh==0) gstc2(u32x2{__float_as_uint(sm),__float_as_uint(qq)},r*16+kh*8,ws+WS_ST+((size_t)np*S+tk*128+tg*32)*16);
    wait_vm<0>(); bar();
    if(w==0 && lane==0) publish(ws,bx,base+3);
    p3pre();
  } else if(ii<27){
    const int lane=lane_id();
    const int m=xcd&1, cb=(ii<26)?8*(xcd>>1)+ii-18:32+(xcd>>1);
    const char* src=(m?wu:wg)+cb*256+(size_t)w*72*(F*2);
    u32x4 v[18], sel;
    #pragma unroll
    for(int i=0;i<18;i++) gld(v[i],(lane>>4)*(F*2)+(lane&15)*16,src+(size_t)i*4*(F*2));
    { const unsigned o=(lane&8)?0u:((lane&1)?0x3f800000u:0x00003f80u); const int j=(lane&7)>>1; sel=u32x4{j==0?o:0u,j==1?o:0u,j==2?o:0u,j==3?o:0u}; }
    f32x4 a={};
    static_for<0,9>([&](auto G){ constexpr int g=decltype(G)::value;
      wait_vm2<16-2*g>(v[2*g],v[2*g+1]); a=MFMA16(BF8(sel),BF8(v[2*g]),a); a=MFMA16(BF8(sel),BF8(v[2*g+1]),a); });
    LAS float* red=(LAS float*)(lds+65536);
    if(lane<32) *(LAS f32x4*)(red+w*128+(lane&15)*8+(lane>>4)*4)=a;
    wait_lgkm0(); bar();
    if(w<2){ const int t_=w*64+lane; float s=0.f;
      #pragma unroll
      for(int i=0;i<8;i++) s+=red[i*128+t_];
      const unsigned hi=pk2(s,s)&0xffffu, lo=pk2(s-lo16(hi),0.f)&0xffffu;
      const int f=cb*128+t_; int j,t,l;
      if(f<4096){ j=f>>7; t=((f>>5)&3)*2+m; l=f&31; } else { j=(f-4096)>>4; t=8; l=(f&15)+16*m; }
      gstc1(hi|(lo<<16),(((j*3+(t>>2))*32+l)*4+(t&3))*4,ws+WS_CS); wait_vm<0>(); }
    bar();
    if(w==0 && lane==0) publish(ws,bx,base+3);
    p3pre();
  } else {
    p3pre();
    const int lane=lane_id(), sk=xcd>>1;
    u32x4 v[16];
    #pragma unroll
    for(int i=0;i<16;i++){ const int o=((((xcd&1)*5+ii-27)*8+w)*16+i)*1024;
      gld(v[i],lane*16,wd+(o<1024*1152?(size_t)sk*1024*1152+o:(size_t)(4096+128*sk)*1152+(o-1024*1152))); }
    wait_vm<0>();
    #pragma unroll
    for(int i=0;i<16;i++) pin(v[i]);
  }
  const int sk=xcd>>1, t4=8*(xcd&1)+(ii&7), ng=ii>>3;
  unsigned vW4, pl4, tW4, tH4, oW4=0, oH4=0;
  auto mk4=[&](){ const int l=lane_id(); vW4=(unsigned)((8*(w&1)+(l>>3))*(D*2)+(((l&7)^(((l>>4)&1)<<2))*16));
    auto fs=[&](int i){ const int c=l-12+3*(ng&1)+(ng&2), c6=c>=6?c-6:c;
      return l<8?64*sk+8*((5*ng+l)&7)+i:(l<12||c6<4)?64*sk+16*(l<12?(l+ng)&3:c6)+8*(i>>1)+(l<12?4:6)+(i&1):240+8*sk+4*c6+i; };
    tW4=fs(w>>1)*(16*D*2); tH4=fs(2*(w>>2))*1024; };
  const char* w4a=wd+ng*256; const char* w4e=wd+1024; const char* h4=ws+WS_G+(size_t)(t4*4+(w&3))*288*1024;
  const unsigned l4w=lb+w*1024, l4h=lb+16384+((w&3)*4+2*(w>>2))*1024;
  auto ld4=[&](auto P,auto O,unsigned l16){ constexpr int p=decltype(P)::value, o=decltype(O)::value;
    if constexpr(o==0){ oH4=addlane<p>(tH4,l16); gldsx<slot4(p),0,true>(h4,oH4,l4h); }
    if constexpr(o==1) gldsx<slot4(p)+1024,1024,true>(h4,oH4,l4h);
    if constexpr(o==2){ oW4=addlane<p>(tW4,vW4); gldsx<slot4(p),0,false>(w4a,oW4,l4w); }
    if constexpr(o==3) gldsx<slot4(p)+8192,128,false>(w4a,oW4,l4w);
    if constexpr(o==4) gldsx<p==13?147456:106496,0,false>(w4e,oW4,l4w); };
  auto ask4=[&](){ gld1c(pl4,(((lane_id()&7)*4+((t4>>1)&3))*8+xcd)*128,ws+WS_FLAG); };
  auto got4=[&](unsigned want){ pin(pl4); if(!__all(pl4-base-want<8u-want)) wait_progress(ws,((lane_id()&7)*4+((t4>>1)&3))*8+xcd,8,base,want); };
  {
    const int lane=lane_id();
    const int tt=4*(xcd&1)+(ii&3), j=8*(xcd>>1)+(ii>>2), T=tt*8+w;
    const unsigned voffS=(unsigned)((8*w+(lane>>3))*(F*2)+(((lane&7)^(((lane>>4)&1)<<2))*16));
    const unsigned xoff=lane*16; const char* xb=ws+WS_H+(size_t)T*36*1024;
    u32x4 x_[36], sp[5], cv[3];
    auto ldw=[&](int m,int kb,int ch,int ps){ glds16((m?uA:gA)+ch*128+(size_t)kb*64*F*2, voffS, lb+ps*16384+m*8192+w*1024); };
    if(w==0){
      int who; if(lane<18){ const int u=(lane>>1)*4+((2*tt+(lane&1))&3); who=(u%18)*8+(tt>>1)*2+u/18; } else who=((lane&2)?26:18+(j&7))*8+(xcd&6)+(lane&1);
      wait_progress(ws,who,22,base,3); }
    bar();
    #pragma unroll
    for(int i=0;i<4;i++) gldc(x_[i],xoff,xb+i*1024);
    bar();
    #pragma unroll
    for(int i=4;i<8;i++) gldc(x_[i],xoff,xb+i*1024);
    auto lds_=[&](int i){ const unsigned l16=(lane_id()&31)*16;
      if(i<4) gldc(sp[i],l16+(lane_id()>>5)*(4*S*16),ws+WS_ST+(size_t)T*32*16+i*(S*16)); else if(i==4) gldc(sp[4],l16,ws+WS_ST+(size_t)T*32*16+8*S*16); else gldc(cv[i-5],l16,ws+WS_CS+j*1536+(i-5)*512); };
    if(w>=4) __builtin_amdgcn_s_setprio(1);
    const int q=(lane&15)>>2, p=lane&3, g2=(lane>>4)&1, hh=lane>>5;
    const unsigned a0_=(4*hh+q)*128+(((2*g2+(p>>1))^((q>>1)<<2))*16)+(p&1)*8, a0=lb+a0_, a1=lb+(a0_^64);
    unsigned ac=0;
    auto ldc=[&](int pi,int sl){ const unsigned l4=lane_id(); const int row0=pi*128+16*w-((pi==4&&w>=4)?64:0);
      const char* m=((l4&2)?wu:wg)+(size_t)(4096+16*j)*2+(size_t)row0*(F*2);
      glds16v(m+((l4>>2)*(F*2)+(l4&1)*16), lb+sl*8192+w*1024); };
    f32x16 acc[4]={};
    char* outb=ws+WS_G+(size_t)T*288*1024;
    constexpr int PD=4, NU=324;
    bf16x8 A[PD]; u32x4 eo, bm; float k1, iv, sm;
    auto unit=[](int u,int& kb,int& s,int& t){
      if(u<96){ kb=u>>4; s=(u>>2)&3; t=u&3; }
      else if(u<120){ int v=u-96; kb=6+(v>>3); s=(v>>1)&3; t=v&1; }
      else if(u<144){ int v=u-120; kb=6+(v>>3); s=(v>>1)&3; t=2+(v&1); }
      else if(u<216){ int v=u-144; kb=v>>3; s=(v>>1)&3; t=v&1; }
      else if(u<288){ int v=u-216; kb=v>>3; s=(v>>1)&3; t=2+(v&1); }
      else { int v=u-288; kb=v>>2; s=v&3; t=0; } };
    unsigned pb0=0,pb1=0;
    auto rd=[&](auto U){ constexpr int u=decltype(U)::value; int kb=0,s=0,t=0; unit(u,kb,s,t);
      if constexpr(u<288){ constexpr int kbc=(u<96)?(u>>4):(u<120)?6+((u-96)>>3):(u<144)?6+((u-120)>>3):(u<216)?((u-144)>>3):((u-216)>>3); constexpr int ps=(u<144)?kbc:(kbc+9)%10;
        if constexpr(u<96){ if constexpr((u&15)==0) pb0=addc<ps*16384>(a0); if constexpr((u&15)==2) pb1=addc<ps*16384>(a1); }
        else if constexpr((u&7)==0){ if constexpr(u<120||(u>=144&&u<216)) pb0=addc<ps*16384>(a0); else pb1=addc<ps*16384>(a1); }
        A[u%PD]=ldtr<1024>(((t&2)?pb1:pb0)+(t&1)*8192+s*2048); }
      else { constexpr int v=u-288, pi=v/8, sl=(pi<4)?16+pi:0;
        if constexpr(v==0){ const int l=lane_id(); ac=lb+(4*(l>>5)+((l&15)>>2))*64+(2*((l>>4)&1)+((l&3)>>1))*16+(l&1)*8; }
        if constexpr((v&7)==0) pb0=addc<sl*8192>(ac);
        A[u%PD]=ldtr<512>(pb0+(v%8)*1024); } };
    float ea[2], eb[2], ep;
    auto job=[&](auto I,auto NE,const f32x16& g,const f32x16& u,int uo,int fs){ constexpr int i=decltype(I)::value, ne=decltype(NE)::value;
      constexpr int k=i/3, st=i%3, e=k-st;
      if constexpr(e>=0 && e<ne){
        if constexpr(st==0) ea[e&1]=__builtin_amdgcn_exp2f(g[e]*k1);
        if constexpr(st==1) eb[e&1]=__builtin_amdgcn_rcpf(fmaf(ea[e&1],iv,iv));
        if constexpr(st==2){ float v=opf(g[e]*u[uo+e]*eb[e&1]);
          if constexpr((e&1)==0) ep=v; else eo[(e>>1)&3]=pk2(ep,v);
          if constexpr((e&7)==7) gstc(eo,xoff,outb+(fs+(e>>3))*1024); } } };
    auto spread=[&](auto U,auto U0,auto L,const f32x16& g,const f32x16& uu,int fs){ constexpr int u=decltype(U)::value,u0=decltype(U0)::value,l=decltype(L)::value;
      if constexpr(u>=u0 && u<u0+l){ constexpr int b=(u-u0)*54/l, e=(u-u0+1)*54/l; static_for<b,e>([&](auto I){ job(I,IC(16),g,uu,0,fs); }); } };
    auto corr=[&](unsigned d,const f32x16& c){ u32x4 a={d,d&0xffffu,0u,0u}; return MFMA32(BF8(a),BF8(bm),c); };
    static_for<-PD,NU>([&](auto U){ constexpr int u=decltype(U)::value; constexpr int r=u+PD;
      if constexpr(r<=96 && (r&15)==0){ constexpr int kb=r>>4;
        constexpr int n=(kb==0)?4:(kb==1)?8:(kb==2)?12:(kb==3)?14:(kb==4)?11:(kb==5)?12:14;
        wait_vm4<n>(x_[4*kb],x_[4*kb+1],x_[4*kb+2],x_[4*kb+3]);
        bar(); }
      if constexpr(r==104){ wait_vm4<11>(x_[28],x_[29],x_[30],x_[31]); bar(); }
      if constexpr(r==112){ wait_vm4<6>(x_[32],x_[33],x_[34],x_[35]); bar(); }
      if constexpr(r==144){ wait_vm<3>(); bar(); }
      if constexpr(r==144+40){ wait_vm<2>(); bar(); }
      if constexpr(r==288){ wait_vm<1>(); bar(); }
      if constexpr(u>=0){ int kb=0,s=0,t=0; unit(u,kb,s,t);
        if constexpr(u==144||u==145) acc[u-144]=corr(cv[1][u-144],f32x16{});
        if constexpr(u==216||u==217) acc[u-214]=corr(cv[1][2+u-216],f32x16{});
        if constexpr(u==288) acc[0]=corr(cv[2][0],f32x16{});
        if constexpr(u>=88&&u<92) acc[u-88]=corr(cv[0][u-88],acc[u-88]);
        acc[t]=MFMA32(A[u%PD],BF8(x_[u<288?4*kb+s:u-288]),acc[t]);
        SB(); }
      if constexpr(r<NU) rd(IC(r));
      if constexpr(r<96){ if constexpr((r&1)==0){ constexpr int kb=r>>4, sl=(r&15)>>1;
          constexpr int op[6][8]={{8,9,10,11,-1,-2,12,13},{14,15,-1,-2,16,17,18,19},{40,41,42,43,44,-1,-2,45},{20,21,22,23,-1,-2,24,25},{26,27,-1,-2,28,29,30,31},{-1,-2,32,33,34,35,46,47}}, o=op[kb][sl];
          if constexpr(o<0) ldw(-1-o,kb+3,0,kb+3); else if constexpr(o<40) gldc(x_[o],xoff,xb+o*1024); else lds_(o-40); } }
      else if constexpr(r<128){ constexpr int o=(r-96)&7; constexpr int slot=(o==1)?0:(o==3)?1:(o==6)?2:-1;
        if constexpr(slot>=0){ constexpr int n=((r-96)>>3)*3+slot, i=n>>1; ldw(n&1,i,1,i==0?9:i-1); } }
      if constexpr(r==130) ldw(0,6,1,5);
      if constexpr(r==133) ldw(1,6,1,5);
      if constexpr(r==80) pin(cv[0]);
      if constexpr(r==112){ pin(cv[1]); pin(cv[2]); }
      if constexpr(r>=64 && r<76){
        if constexpr(r==64){ pin(sp[0]); pin(sp[1]); pin(sp[2]); pin(sp[3]); pin(sp[4]); }
        if constexpr(r==65){ const bool h2=lane_id()>=32; sp[4][0]=h2?sp[4][2]:sp[4][0]; sp[4][1]=h2?sp[4][3]:sp[4][1]; float a=__uint_as_float(sp[4][0]);
          #pragma unroll
          for(int i=0;i<4;i++) a+=__uint_as_float(sp[i][0])+__uint_as_float(sp[i][2]);
          sm=xsum32(a)*(1.f/18); }
        if constexpr(r==69){ float d=__uint_as_float(sp[4][0])-sm; float a=__uint_as_float(sp[4][1]), b=d*d;
          #pragma unroll
          for(int i=0;i<8;i++){ d=__uint_as_float(sp[i>>1][(i&1)*2])-sm; a+=__uint_as_float(sp[i>>1][(i&1)*2+1]); b=fmaf(d,d,b); }
          a=fmaf(b,32.f,a);
          iv=xsum32(a)*(1.f/D)+1e-5f; }
        if constexpr(r==74){ k1=__builtin_amdgcn_rsqf(iv)*-1.44269504f;
          const unsigned mh=pk2(-sm,-sm); const float ml=-sm-lo16(mh);
          const bool h2=lane_id()>=32; bm=u32x4{h2?0u:mh, h2?0u:(pk2(ml,ml)&0xffffu), 0u, 0u}; } }
      if constexpr(r==144+1) ldw(0,7,1,6);
      if constexpr(r==144+4) ldw(1,7,1,6);
      if constexpr(r==144+7) ldw(0,8,1,7);
      if constexpr(r==144+10) ldw(1,8,1,7);
      if constexpr(r==144+42) ldc(0,16);
      if constexpr(r==144+46) ldc(1,17);
      if constexpr(u==196){ const int e=(ii*8+w)*41+lane_id(), e2=(lane_id()<41&&e<10368)?e:0, row=e2/9;
        gld1(pl4,(row<1024?1024*sk+row:3072+128*sk+row)*(D*2)+(e2-row*9)*128,wd); }
      if constexpr(u==268){ wait_vm<1>(); pin(pl4); }
      if constexpr(u==216+15){ SB(); wait_vm<3>(); bar(); if(w==0 && lane_id()==0) publish(ws,bx,base+4); }
      if constexpr(u==270) ask4();
      if constexpr(u==216+16) ldc(2,18);
      if constexpr(u==216+19) ldc(3,19);
      if constexpr(u==216+22) ldc(4,0);
      if constexpr(u==289) mk4();
      if constexpr(u==290) got4(4);
      if constexpr(u>=290&&u<323&&((u-290)*2)%5<2){ constexpr int n=((u-290)*2)/5, p=n<5?0:(n-1)>>2, o=n<5?n:(n-1)&3; ld4(IC(p),IC(o),xoff); }
      spread(U,IC(121),IC(23),acc[0],acc[1],8*j);
      spread(U,IC(146),IC(34),acc[2],acc[3],8*j+2);
      spread(U,IC(218),IC(66),acc[0],acc[1],8*j+4);
      spread(U,IC(290),IC(34),acc[2],acc[3],8*j+6);
      SB();
    });
    static_for<0,30>([&](auto I){ job(I,IC(8),acc[0],acc[0],8,256+j); });
  }
  {
    const int lane=lane_id(); const unsigned l16=lane*16;
    const int tg=w&3, ch=w>>2;
    const int q=(lane&15)>>2, p=lane&3, g2=(lane>>4)&1, hh=lane>>5;
    const unsigned a0_=(4*hh+q)*128+(((2*g2+(p>>1))^((q>>1)<<2))*16)+(p&1)*8, a0=lb+ch*8192+a0_, a1=lb+ch*8192+(a0_^64), aE=lb+106496+(a0_^(ch*64)), aH=lb+16384+tg*4096+l16;
    f32x16 c[3]={};
    constexpr int PD=4, NU=164;
    bf16x8 A[PD], B[4]; unsigned pb0=0,pb1=0,pbH=0;
    const unsigned keep=ng<2?~0u:0u;
    static_for<-PD,NU>([&](auto U){ constexpr int u=decltype(U)::value, r=u+PD;
      constexpr int pr=stg(r), orr=r-st0(pr), nr=2+ex4(pr);
      if constexpr(r<NU && orr==0){
        constexpr int n=(pr<16?4+ex4(pr+2):0)+(pr<17?4+ex4(pr+1):0);
        wait_vm<n>(); bar();
        if constexpr(pr==0){ if(w==0 && lane==0) publish(ws,bx,base+5); }
        if constexpr(pr==2){ if(w==0 && lane==0) publish(ws,bx,base+6); }
        if constexpr(pr==5) got4(5);
        if constexpr(pr==9) got4(6);
        pb0=addc<slot4(pr)>(a0); pb1=addc<slot4(pr)>(a1); pbH=addc<slot4(pr)>(aH); }
      if constexpr(u>=0){ constexpr int pu=stg(u), ou=u-st0(pu), nu=2+ex4(pu), s=ou/nu, t=ou%nu;
        if constexpr(pu==13&&t==2){ u32x4 b=__builtin_bit_cast(u32x4,B[s]); b&=keep; c[2]=MFMA32(A[u%PD],BF8(b),c[2]); }
        else c[t]=MFMA32(A[u%PD],B[s],c[t]);
        SB(); }
      if constexpr(r<NU){ constexpr int s=orr/nr, t=orr%nr;
        if constexpr(t==0) B[s]=*(LAS bf16x8*)(size_t)(pbH+s*1024);
        A[r%PD]=ldtr<1024>((t==0?pb0:t==1?pb1:aE)+s*2048+((t==2&&pr==13)?40960:0)); }
      if constexpr(u==st0(14)+3||u==st0(14)+6){ constexpr int hf=u==st0(14)+6; gstc(__builtin_bit_cast(u32x4,pack8(c[2],8*hf)),l16,ws+WS_R+(size_t)((t4*16+tg*4+ch*2+hf)*16+sk*4+ng)*1024); }
      if constexpr(r<NU && pr+3<18){
        if constexpr((pr==3||pr==7)&&orr==0) ask4();
        if constexpr(orr==1||orr==3) ld4(IC(pr+3),IC(orr>>1),l16);
        if constexpr(orr==5||orr==6||(orr==7&&ex4(pr+3))) ld4(IC(pr+3),IC(orr-3),l16); }
      SB();
    });
    __builtin_amdgcn_s_setprio(0);
    char* xb=ws+WS_X+(size_t)((t4*4+ng)*4+tg)*4*8192+ch*4096;
    char* rb=ws+WS_R+(size_t)t4*256*1024;
    if(tg!=sk){ char* d=xb+sk*8192;
      gstc(__builtin_bit_cast(u32x4,pack8(c[0],0)),l16,d); gstc(__builtin_bit_cast(u32x4,pack8(c[0],8)),l16,d+1024);
      gstc(__builtin_bit_cast(u32x4,pack8(c[1],0)),l16,d+2048); gstc(__builtin_bit_cast(u32x4,pack8(c[1],8)),l16,d+3072);
    } else {
      #pragma unroll
      for(int i=0;i<8;i++) *(LAS f32x4*)(lds+73728+ch*8192+i*1024+l16)=f32x4{c[i>>2][(4*i)&15],c[i>>2][(4*i+1)&15],c[i>>2][(4*i+2)&15],c[i>>2][(4*i+3)&15]}; }
    u32x4 rr; gldc(rr,l16,ws+WS_H+((size_t)(t4*4+sk)*36+ng*8+w)*1024);
    wait_vm<0>(); wait_lgkm0(); bar();
    if(w==0 && lane==0) publish(ws,bx,base+7);
    const f32x4 m0=*(LAS f32x4*)(lds+73728+w*2048+l16), m1=*(LAS f32x4*)(lds+73728+w*2048+1024+l16);
    auto quarter=[&](){
      u32x4 pv[3];
      #pragma unroll
      for(int s=0;s<3;s++) gldc(pv[s],l16,ws+WS_X+(size_t)(((t4*4+ng)*4+sk)*4+s+(s>=sk?1:0))*8192+w*1024);
      wait_vm3<0>(pv[0],pv[1],pv[2]); pin(rr);
      float v[8];
      #pragma unroll
      for(int e=0;e<4;e++){
        v[2*e]=(e<2?m0[2*e]:m1[2*e-4])+lo16(rr[e])+lo16(pv[0][e])+lo16(pv[1][e])+lo16(pv[2][e]);
        v[2*e+1]=(e<2?m0[2*e+1]:m1[2*e-3])+hi16(rr[e])+hi16(pv[0][e])+hi16(pv[1][e])+hi16(pv[2][e]); }
      const auto s0=__builtin_amdgcn_permlane32_swap(pk2(v[0],v[1]),pk2(v[4],v[5]),false,false), s1=__builtin_amdgcn_permlane32_swap(pk2(v[2],v[3]),pk2(v[6],v[7]),false,false);
      gsto(u32x4{s0[0],s1[0],s0[1],s1[1]},(((t4*4+sk)*32+(lane&31))*D+ng*128+w*16+8*hh)*2,outp);
      };
    if(w!=4){ wait_progress(ws,ii*8+(lane&3)*2+(xcd&1),4,base,7); quarter(); }
    else {
      u32x2 hr[2];
      const int n=(lane&15)+16*hh, hf=(lane>>4)&1;
      { const char* hs=ws+WS_H+((size_t)(t4*4+sk)*36+32+ng)*1024; gld2c(hr[0],n*16+hf*8,hs); gld2c(hr[1],n*16+hf*8,hs+512); }
      wait_progress(ws,((lane>>2&3)*8+(ii&7))*8+(lane&3)*2+(xcd&1),16,base,7);
      u32x4 pv[8][2];
      { const char* d=rb+(sk*4+ng)*16384;
        #pragma unroll
        for(int i=0;i<8;i++){ gldc(pv[i][0],(lane&31)*16+hh*1024,d+i*2048); gldc(pv[i][1],(lane&31)*16+hh*1024,d+i*2048+512); } }
      u32x4 sel; { const unsigned o=(((lane>>3)^(lane>>4))&1)?0u:((lane&1)?0x3f800000u:0x00003f80u); const int j=(lane&7)>>1; sel=u32x4{j==0?o:0u,j==1?o:0u,j==2?o:0u,j==3?o:0u}; }
      quarter();
      f32x4 e0={},e1={};
      #pragma unroll
      for(int i=0;i<8;i++){ pin(pv[i][0]); pin(pv[i][1]); e0=MFMA16(BF8(sel),BF8(pv[i][0]),e0); e1=MFMA16(BF8(sel),BF8(pv[i][1]),e1); }
      pin(hr[0]); pin(hr[1]);
      gsto(u32x4{pk2(e0[0]+lo16(hr[0][0]),e0[1]+hi16(hr[0][0])),pk2(e0[2]+lo16(hr[0][1]),e0[3]+hi16(hr[0][1])),pk2(e1[0]+lo16(hr[1][0]),e1[1]+hi16(hr[1][0])),pk2(e1[2]+lo16(hr[1][1]),e1[3]+hi16(hr[1][1]))},
           (((t4*4+sk)*32+n)*D+512+ng*16+hf*8)*2,outp);
    }
  }
}

extern "C" void kernel_launch(void* const* d_in, const int* in_sizes, int n_in, void* d_out, int out_size, void* d_ws, size_t ws_size, hipStream_t stream){
  (void)hipFuncSetAttribute((const void*)mega,hipFuncAttributeMaxDynamicSharedMemorySize,163840);
  mega<<<256,512,163840,stream>>>((const char*)d_in[0],(const char*)d_in[1],(const char*)d_in[2],(const char*)d_in[3],(const char*)d_in[5],(const char*)d_in[6],(const char*)d_in[7],(char*)d_ws,(char*)d_out);
}
```
